```python
import torch

def _softmax(x, axis=-1):
    return torch.softmax(x, dim=axis)
def _gelu(x):
    return torch.nn.functional.gelu(x, approximate="tanh")
def _silu(x):
    return torch.nn.functional.silu(x)
def _sigmoid(x):
    return torch.sigmoid(x)
def _layernorm(x, axis=-1, eps=1e-5):
    m = x.mean(dim=axis, keepdim=True)
    v = x.var(dim=axis, keepdim=True, unbiased=False)
    return (x - m) / torch.sqrt(v + eps)
def _scatter_add(tgt, idx, src, axis=0):
    return tgt.clone().index_add_(axis, idx, src)
def _segment_sum(src, seg, n):
    out = torch.zeros((n,) + src.shape[1:], dtype=src.dtype)
    return out.index_add_(0, seg, src)
def _pad(x, pw):
    flat = tuple(v for pair in reversed(pw) for v in pair)
    return torch.nn.functional.pad(x, flat)

kernel_name = 'recurrent_scan__sliding_block_cached__dense_activation_squared_relu__decode'

def setup_inputs():

    _w = (lambda a: (lambda t: t.to(torch.bfloat16) if t.is_floating_point() else t)(torch.as_tensor(a)))
    def _A_setup():
        B, Sq, D = 2, 1, 576
        Hq, Hkv, Sk, Dh = 32, 16, 16384, 128
        assert Hq % Hkv == 0, (Hq, Hkv)
        return {
            "x": _w((lambda *s: torch.randn(*s, dtype=torch.bfloat16) * 0.05)(B, Sq, D)),
            "k_cache": _w((lambda *s: torch.randn(*s, dtype=torch.bfloat16) * 0.8)(B, Hkv, Sk, Dh)),
            "v_cache": _w((lambda *s: torch.randn(*s, dtype=torch.bfloat16) * 0.2)(B, Hkv, Sk, Dh)),
            "wq": _w((lambda *s: torch.randn(*s, dtype=torch.bfloat16) * 0.8)(D, Hq * Dh) * (D ** -0.5)),
            "wo": _w((lambda *s: torch.randn(*s, dtype=torch.bfloat16) * 0.2)(Hq * Dh, D) * ((Hq * Dh) ** -0.5)),
        }
    _A_in = _A_setup()

    def _B_setup():
        B, S, D, F = 2, 1, 576, 4608
        return {
            "x": _w((lambda *s: torch.randn(*s, dtype=torch.bfloat16) * 0.05)(B, S, D)),
            "w1": _w((lambda *s: torch.randn(*s, dtype=torch.bfloat16) * 0.1)(D, F)),
            "b1": _w((lambda *s: torch.randn(*s, dtype=torch.bfloat16) * 0.1)(F)),
            "w2": _w((lambda *s: torch.randn(*s, dtype=torch.bfloat16) * 0.1)(F, D)),
            "b2": _w((lambda *s: torch.randn(*s, dtype=torch.bfloat16) * 0.1)(D)),
        }
    _B_in = _B_setup()

    return {
        "A_x": _A_in["x"],
        "A_k_cache": _A_in["k_cache"],
        "A_v_cache": _A_in["v_cache"],
        "A_wq": _A_in["wq"],
        "A_wo": _A_in["wo"],
        "B_x": _B_in["x"],
        "B_w1": _B_in["w1"],
        "B_b1": _B_in["b1"],
        "B_w2": _B_in["w2"],
        "B_b2": _B_in["b2"],
    }

def reference(A_x, A_k_cache, A_v_cache, A_wq, A_wo, B_x, B_w1, B_b1, B_w2, B_b2):
    def _A_ref(x, k_cache, v_cache, wq, wo):
        B, Sq, D = x.shape
        Hq, Dh = 32, 128
        Hkv, Sk = k_cache.shape[1], k_cache.shape[2]
        W = min(4096, max(256, Sk // 4))
        rep = Hq // Hkv
        q = (lambda x, *d: x.permute(*d))((x @ wq).reshape(B, Sq, Hq, Dh), 0, 2, 1, 3)
        k = (lambda x, n, axis: torch.repeat_interleave(x, n, dim=axis))(k_cache[:, :, Sk - W:, :], rep, axis=1)
        v = (lambda x, n, axis: torch.repeat_interleave(x, n, dim=axis))(v_cache[:, :, Sk - W:, :], rep, axis=1)
        scores = torch.einsum("bhqd,bhkd->bhqk", q, k) * (Dh ** -0.5)
        p = _softmax(scores, axis=-1)
        o = torch.einsum("bhqk,bhkd->bhqd", p, v)
        return (lambda x, *d: x.permute(*d))(o, 0, 2, 1, 3).reshape(B, Sq, Hq * Dh) @ wo

    def _B_ref(x, w1, b1, w2, b2):
        h = torch.einsum("bsd,df->bsf", x, w1) + b1
        zero = torch.zeros_like(h)
        r = torch.where(h > zero, h, zero)
        h = r * r
        out = torch.einsum("bsf,fd->bsd", h, w2) + b2
        return out

    x = A_x
    xn = _layernorm(x, axis=-1)
    s = _A_ref(xn, A_k_cache, A_v_cache, A_wq, A_wo)
    g = _silu(_B_ref(xn, B_w1, B_b1, B_w2, B_b2))
    return x + s * g
```

```cpp
#include <hip/hip_runtime.h>
#include <stdint.h>

typedef unsigned short u16;
typedef unsigned v4u __attribute__((ext_vector_type(4)));
typedef float f32x4 __attribute__((ext_vector_type(4)));
typedef short bf16x8 __attribute__((ext_vector_type(8)));
typedef __bf16 bf16x2 __attribute__((ext_vector_type(2)));
#define LAS __attribute__((address_space(3)))
#define B0MODE 1

constexpr int D = 576, HKV = 16, SK = 16384, DH = 128, WIN = 4096, F = 4608;
constexpr int WS_E = 0;
constexpr int WS_QP = 4096;
constexpr int WS_HP = WS_QP + 16 * 2 * 4 * 128 * 16;
constexpr int WS_AP = WS_HP + 72 * 2 * 3 * 32 * 16;
constexpr int AP_REC = 136 * 16;
constexpr int WS_SP = WS_AP + 256 * AP_REC;
constexpr int WS_MP = WS_SP + 9 * 2 * 16 * 32 * 16;
constexpr int WS_END = WS_MP + 9 * 2 * 12 * 32 * 16;
constexpr int WS_STAMP = (WS_END + 4095) / 4096 * 4096;
constexpr unsigned SPIN_CAP = 1u << 18;
constexpr int OOB = 0x7ffffff0;

#define RSRC(p, n) __builtin_amdgcn_make_buffer_rsrc((void*)(p), 0, (n), 0x00020000)
#define LDNT(rs, off) __builtin_amdgcn_raw_buffer_load_b128(rs, off, 0, 2)
#define LDSC(rs, off) __builtin_amdgcn_raw_buffer_load_b128(rs, off, 0, 16)
#define STSC(v, rs, off) __builtin_amdgcn_raw_buffer_store_b128(v, rs, off, 0, 16)

__device__ __forceinline__ float bf2f(unsigned b) { return __uint_as_float(b << 16); }
typedef float f32x2 __attribute__((ext_vector_type(2)));
__device__ __forceinline__ unsigned cvtpk(float lo, float hi) { const f32x2 v = {lo, hi}; return __builtin_bit_cast(unsigned, __builtin_convertvector(v, bf16x2)); }
__device__ __forceinline__ float rbf(float f) { return __uint_as_float(cvtpk(f, f) << 16); }
__device__ __forceinline__ float dot2(unsigned k, unsigned q, float acc) {
    return __builtin_amdgcn_fdot2_f32_bf16(__builtin_bit_cast(bf16x2, k), __builtin_bit_cast(bf16x2, q), acc, false);
}
template <int CTRL> __device__ __forceinline__ float dpp(float x) {
    return __builtin_bit_cast(float, __builtin_amdgcn_mov_dpp(__builtin_bit_cast(int, x), CTRL, 0xf, 0xf, true));
}
__device__ __forceinline__ f32x4 mfma(v4u a, v4u b, f32x4 c) {
    return __builtin_amdgcn_mfma_f32_16x16x32_bf16(__builtin_bit_cast(bf16x8, a), __builtin_bit_cast(bf16x8, b), c, 0, 0, 0);
}
__device__ __forceinline__ float xrow_sum(float x) {
    auto s = __builtin_amdgcn_permlane16_swap(__float_as_uint(x), __float_as_uint(x), false, false);
    x = __uint_as_float(s[0]) + __uint_as_float(s[1]);
    auto t = __builtin_amdgcn_permlane32_swap(__float_as_uint(x), __float_as_uint(x), false, false);
    return __uint_as_float(t[0]) + __uint_as_float(t[1]);
}
__device__ __forceinline__ float xrow_max(float x) {
    auto s = __builtin_amdgcn_permlane16_swap(__float_as_uint(x), __float_as_uint(x), false, false);
    x = fmaxf(__uint_as_float(s[0]), __uint_as_float(s[1]));
    auto t = __builtin_amdgcn_permlane32_swap(__float_as_uint(x), __float_as_uint(x), false, false);
    return fmaxf(__uint_as_float(t[0]), __uint_as_float(t[1]));
}
__device__ __forceinline__ float wave_sum(float v) {
    v += dpp<0xB1>(v); v += dpp<0x4E>(v); v += dpp<0x141>(v); v += dpp<0x140>(v);
    return xrow_sum(v);
}
__device__ __forceinline__ bool tag_ok(v4u p, unsigned T) { return (p.y == T) & (p.w == ~T); }
__device__ __forceinline__ v4u mkpair(float a, float b, unsigned T) { return v4u{__float_as_uint(a), T, __float_as_uint(b), ~T}; }

#define SPIN_PAUSE(n) do { __builtin_amdgcn_s_sleep(n); asm volatile("" :: "s"(ws) : "memory"); } while (0)
#define STAMPW(i, cond) do { } while (0)

constexpr int L_XS = 0;
constexpr int L_Q = 4608;
constexpr int L_ACT = 5120;
constexpr int L_ML = 6656;
constexpr int L_GML = 6784;
constexpr int L_FLAG = 7040;
constexpr int L_SCR = 7168;
constexpr int L_RED2 = 9216;
constexpr int L_RED = 13312;
constexpr int L_BYTES = L_RED + 16384;

__global__ void __launch_bounds__(768, 3)
fused_kernel(const u16* __restrict__ x, const u16* __restrict__ kc, const u16* __restrict__ vc, const u16* __restrict__ wq, const u16* __restrict__ wo,
             const u16* __restrict__ w1, const u16* __restrict__ b1, const u16* __restrict__ w2, const u16* __restrict__ b2,
             u16* __restrict__ out, unsigned char* ws) {
    __shared__ __attribute__((aligned(16))) unsigned char lds[L_BYTES];
    const int tid = threadIdx.x, lane = tid & 63, w = __builtin_amdgcn_readfirstlane(tid >> 6), bid = blockIdx.x;
    STAMPW(0, w == 0);
    const int s = bid & 7, b = (bid >> 3) & 1, kvh = bid >> 4;
    const int kq = (bid & 15) >> 2, cq = bid & 3;
    const bool has_w1 = bid < 216;
    const int c1 = bid % 72, k1 = bid / 72;
    const bool role_wo = bid < 144, role_w2 = bid >= 144 && bid < 252;
    const int u2 = bid - 144;
    const int tcg = role_wo ? bid % 9 : u2 % 9;
    const int trg = role_wo ? bid / 9 : u2 / 9;
    const int trows = role_wo ? 256 : 384;
    const __amdgpu_buffer_rsrc_t rws = RSRC(ws, WS_END);
    volatile LAS unsigned* qflag = (volatile LAS unsigned*)(LAS unsigned char*)(lds + L_FLAG);
    if (tid == 0) *qflag = 0u;

    const int lrow = lane >> 3, lcol = (lane & 7) * 16;
    const unsigned hm = (lane & 1) ? 0xffff0000u : 0x0000ffffu;
    const int dsel = (lane >> 1) & 3;
    const unsigned M0 = dsel == 0 ? hm : 0u, M1 = dsel == 1 ? hm : 0u, M2 = dsel == 2 ? hm : 0u, M3 = dsel == 3 ? hm : 0u;
#define BDIAG(dup) v4u{(dup) & M0, (dup) & M1, (dup) & M2, (dup) & M3}
    unsigned T;

    if (w < 8) {
        const __amdgpu_buffer_rsrc_t rk = RSRC(kc + ((size_t)(b * HKV + kvh) * SK + (SK - WIN) + s * 512 + w * 64) * DH, 64 * 256);
        const __amdgpu_buffer_rsrc_t rv = RSRC(vc + ((size_t)(b * HKV + kvh) * SK + (SK - WIN) + s * 512 + w * 64) * DH, 64 * 256);
        v4u K[16], V[16];
        __syncthreads();
        __builtin_amdgcn_s_setprio(1);
#pragma unroll
        for (int j = 0; j < 16; ++j) K[j] = LDNT(rk, j * 1024 + lane * 16);
        __builtin_amdgcn_sched_barrier(0);
        __builtin_amdgcn_s_setprio(0);
        __builtin_amdgcn_sched_barrier(0);
#pragma unroll
        for (int j = 0; j < 16; ++j) V[j] = LDNT(rv, j * 1024 + lane * 16);
        __builtin_amdgcn_sched_barrier(0);
        STAMPW(1, w == 0); STAMPW(2, w == 7);
        { unsigned spin = 0; while (*qflag == 0u && ++spin < (SPIN_CAP << 4)) __builtin_amdgcn_s_sleep(1); T = qflag[1]; }
        asm volatile("" ::: "memory");
        STAMPW(3, w == 0); STAMPW(4, w == 7);
        const int c = lane & 15;
        const v4u q0 = *(const v4u*)(lds + L_Q + c * 16), q1 = *(const v4u*)(lds + L_Q + 256 + c * 16);
        float sc[16];
        const bool hi = (c & 8) != 0;
#pragma unroll
        for (int t = 0; t < 16; ++t) {
            float d0 = 0.f, d1 = 0.f;
#pragma unroll
            for (int k = 0; k < 4; ++k) { d0 = dot2(K[t][k], q0[k], d0); d1 = dot2(K[t][k], q1[k], d1); }
            const float send = hi ? d0 : d1, keep = hi ? d1 : d0;
            float v = keep + dpp<0x128>(send);
            v += dpp<0xB1>(v); v += dpp<0x4E>(v); v += dpp<0x141>(v);
            sc[t] = rbf(rbf(v) * 0.088388347648318440f);
        }
        float m = sc[0];
#pragma unroll
        for (int t = 1; t < 16; ++t) m = fmaxf(m, sc[t]);
        m = xrow_max(m);
        float l = 0.f;
        constexpr float LOG2E = 1.4426950408889634f;
        const float mm = m * LOG2E;
        unsigned pd[16];
#pragma unroll
        for (int t = 0; t < 16; ++t) { const float p = __builtin_amdgcn_exp2f(sc[t] * LOG2E - mm); l += p; pd[t] = cvtpk(p, p); }
        l = xrow_sum(l);
        STAMPW(5, w == 7);
        f32x4 acc0 = {0, 0, 0, 0}, acc1 = {0, 0, 0, 0};
#pragma unroll
        for (int t = 0; t < 16; t += 2) { acc0 = mfma(V[t], BDIAG(pd[t]), acc0); acc1 = mfma(V[t + 1], BDIAG(pd[t + 1]), acc1); }
        acc0 += acc1;
        float* red = (float*)(lds + L_RED);
        const int hh = (lane >> 3) & 1;
#pragma unroll
        for (int r = 0; r < 4; ++r) red[(w * 2 + hh) * 128 + 8 * (4 * (lane >> 4) + r) + (lane & 7)] = acc0[r];
        if ((lane & 0x37) == 0) { float* ml = (float*)(lds + L_ML); ml[(w * 2 + hh) * 2] = mm; ml[(w * 2 + hh) * 2 + 1] = l; }
        __syncthreads();
        STAMPW(6, w == 0);
        if (tid < 130) {
            const float* ml = (const float*)(lds + L_ML);
            const int h2 = tid < 128 ? tid >> 6 : tid - 128, pr = tid & 63;
            float M = ml[h2 * 2];
#pragma unroll
            for (int i = 1; i < 8; ++i) M = fmaxf(M, ml[(i * 2 + h2) * 2]);
            float a0 = 0.f, a1 = 0.f, L = 0.f;
#pragma unroll
            for (int i = 0; i < 8; ++i) {
                const float wt = __builtin_amdgcn_exp2f(ml[(i * 2 + h2) * 2] - M);
                const float2 t = *(const float2*)&red[(i * 2 + h2) * 128 + 2 * pr];
                a0 += wt * t.x; a1 += wt * t.y; L += wt * ml[(i * 2 + h2) * 2 + 1];
            }
            if (tid >= 128) { a0 = M; a1 = L; }
            STSC(mkpair(a0, a1, T), rws, WS_AP + bid * AP_REC + tid * 16);
        }
        STAMPW(7, w == 0);
        if (role_wo && w < 4) {
            const int bb = w >> 1, h2 = w & 1;
            const int base = WS_AP + (trg * 16 + bb * 8) * AP_REC;
            v4u g[8], gm[8];
            bool ok;
            unsigned spin = 0;
            do {
#pragma unroll
                for (int i = 0; i < 8; ++i) { g[i] = LDSC(rws, base + i * AP_REC + (h2 * 64 + lane) * 16); gm[i] = LDSC(rws, base + i * AP_REC + (128 + h2) * 16); }
                ok = true;
#pragma unroll
                for (int i = 0; i < 8; ++i) ok &= tag_ok(g[i], T) & tag_ok(gm[i], T);
                if (__all(ok)) break;
                SPIN_PAUSE(1);
            } while (++spin < SPIN_CAP);
            STAMPW(8, w == 0);
            float M = __uint_as_float(gm[0].x);
#pragma unroll
            for (int i = 1; i < 8; ++i) M = fmaxf(M, __uint_as_float(gm[i].x));
            float a0 = 0.f, a1 = 0.f, L = 0.f;
#pragma unroll
            for (int i = 0; i < 8; ++i) {
                const float wt = __builtin_amdgcn_exp2f(__uint_as_float(gm[i].x) - M);
                a0 += wt * __uint_as_float(g[i].x); a1 += wt * __uint_as_float(g[i].z); L += wt * __uint_as_float(gm[i].z);
            }
            ((unsigned*)(lds + L_ACT))[bb * 192 + h2 * 64 + lane] = cvtpk(a0 / L, a1 / L);
        }
        __syncthreads();
        __syncthreads();
    } else {
        const int sw = w - 8;
        unsigned ep;
        const __amdgpu_buffer_rsrc_t rt = RSRC(role_wo ? wo : w2, role_wo ? 4096 * D * 2 : (role_w2 ? F * D * 2 : 0));
        if (sw < 2) {
            __builtin_amdgcn_s_setprio(3);
            const __amdgpu_buffer_rsrc_t rx = RSRC(x, 2 * D * 2);
            const __amdgpu_buffer_rsrc_t rw = sw == 0 ? RSRC(wq + (size_t)(144 * kq) * 4096 + 256 * kvh + 64 * cq, 144 * 4096 * 2 - 256 * kvh * 2 - 128 * cq)
                                                      : RSRC(w1 + (size_t)(192 * k1) * F + 64 * c1, has_w1 ? 192 * F * 2 - 128 * c1 : 0);
            const int ld = sw == 0 ? 4096 * 2 : F * 2;
            const int row0 = sw == 0 ? 144 * kq : 192 * k1;
            const int nld = sw == 0 ? 18 : 24;
            v4u xr[3];
#pragma unroll
            for (int j = 0; j < 3; ++j) xr[j] = __builtin_amdgcn_raw_buffer_load_b128(rx, (lane + 64 * j) * 16, 0, 0);
            v4u Wf[24];
#pragma unroll
            for (int j = 0; j < 24; ++j) Wf[j] = LDNT(rw, j < nld ? (8 * j + lrow) * ld + lcol : OOB);
            ep = __builtin_amdgcn_raw_buffer_load_b32(rws, WS_E, 0, 16);
            __builtin_amdgcn_sched_barrier(0);
            asm volatile("s_waitcnt vmcnt(25)" ::: "memory");
            __syncthreads();
            __builtin_amdgcn_sched_barrier(0);
            unsigned char* xsb = lds + L_XS + sw * 2304;
            {
                float xv[3][8];
#pragma unroll
                for (int j = 0; j < 3; ++j)
#pragma unroll
                    for (int k = 0; k < 4; ++k) { xv[j][2 * k] = bf2f(xr[j][k] & 0xffffu); xv[j][2 * k + 1] = __uint_as_float(xr[j][k] & 0xffff0000u); }
                float sj[3];
#pragma unroll
                for (int j = 0; j < 3; ++j) { float t = 0.f;
#pragma unroll
                    for (int k = 0; k < 8; ++k) t += xv[j][k];
                    sj[j] = t; }
                const bool c1b0 = lane < 8, c2ok = lane < 16;
                const float mean0 = wave_sum(sj[0] + (c1b0 ? sj[1] : 0.f)) * (1.f / D);
                const float mean1 = wave_sum((c1b0 ? 0.f : sj[1]) + sj[2]) * (1.f / D);
                const float mj[3] = {mean0, c1b0 ? mean0 : mean1, mean1};
#pragma unroll
                for (int j = 0; j < 3; ++j) { float t = 0.f;
#pragma unroll
                    for (int k = 0; k < 8; ++k) { const float d = xv[j][k] - mj[j]; t += d * d; }
                    sj[j] = t; }
                if (!c2ok) sj[2] = 0.f;
                const float var0 = wave_sum(sj[0] + (c1b0 ? sj[1] : 0.f)) * (1.f / D);
                const float var1 = wave_sum((c1b0 ? 0.f : sj[1]) + sj[2]) * (1.f / D);
                const float mb0 = rbf(mean0), mb1 = rbf(mean1);
                const float den0 = rbf(sqrtf(rbf(rbf(var0) + 1e-5f))), den1 = rbf(sqrtf(rbf(rbf(var1) + 1e-5f)));
                const float ri0 = 1.f / den0, ri1 = 1.f / den1;
                const float mbj[3] = {mb0, c1b0 ? mb0 : mb1, mb1}, dnj[3] = {ri0, c1b0 ? ri0 : ri1, ri1};
#pragma unroll
                for (int j = 0; j < 3; ++j) {
                    v4u o;
#pragma unroll
                    for (int k = 0; k < 4; ++k) o[k] = cvtpk(rbf(xv[j][2 * k] - mbj[j]) * dnj[j], rbf(xv[j][2 * k + 1] - mbj[j]) * dnj[j]);
                    if (j < 2 || c2ok) *(v4u*)(xsb + (lane + 64 * j) * 16) = o;
                }
            }
            __builtin_amdgcn_fence(__ATOMIC_ACQ_REL, "wavefront");
            asm volatile("" ::: "memory");
            STAMPW(10, sw == 0);
            T = __builtin_amdgcn_readfirstlane(ep) + 1u;
            const u16* xs = (const u16*)xsb;
            f32x4 af[2] = {{0, 0, 0, 0}, {0, 0, 0, 0}};
#pragma unroll
            for (int j = 0; j < 24; ++j) {
                const int r = min(row0 + 8 * j + lrow, D - 1);
#pragma unroll
                for (int bb = 0; bb < 2; ++bb) { const unsigned v = xs[bb * D + r]; const unsigned dup = v | (v << 16); af[bb] = mfma(Wf[j], BDIAG(dup), af[bb]); }
            }
            float* scr = (float*)(lds + L_SCR + sw * 1024);
            if ((lane >> 5) == ((lane >> 3) & 1)) {
                const int r1 = lane >> 5, cb = 32 * ((lane >> 4) & 1) + (lane & 7);
#pragma unroll
                for (int bb = 0; bb < 2; ++bb)
#pragma unroll
                    for (int r = 0; r < 4; ++r) scr[(r1 * 2 + bb) * 64 + cb + 8 * r] = af[bb][r];
            }
            __builtin_amdgcn_fence(__ATOMIC_ACQ_REL, "wavefront");
            asm volatile("" ::: "memory");
            {
                const int bb = lane >> 5, pr = lane & 31;
                const float2 t0 = *(const float2*)&scr[bb * 64 + 2 * pr], t1 = *(const float2*)&scr[(2 + bb) * 64 + 2 * pr];
                const int off = sw == 0 ? WS_QP + (((kvh * 2 + bb) * 4 + kq) * 128 + cq * 32 + pr) * 16
                                        : (has_w1 ? WS_HP + (((c1 * 2 + bb) * 3 + k1) * 32 + pr) * 16 : OOB);
                STSC(mkpair(t0.x + t1.x, t0.y + t1.y, T), rws, off);
            }
            STAMPW(11, sw == 0);
            if (sw == 0) {
                const int qoff = WS_QP + ((kvh * 2 + b) * 4 * 128 + lane) * 16;
                v4u gq[8]; bool ok; unsigned spin = 0;
                do {
#pragma unroll
                    for (int i = 0; i < 8; ++i) gq[i] = LDSC(rws, qoff + (i >> 1) * 128 * 16 + (i & 1) * 64 * 16);
                    ok = true;
#pragma unroll
                    for (int i = 0; i < 8; ++i) ok &= tag_ok(gq[i], T);
                    if (__all(ok)) break;
                    SPIN_PAUSE(1);
                } while (++spin < SPIN_CAP);
#pragma unroll
                for (int hf2 = 0; hf2 < 2; ++hf2) {
                    const float q0 = (__uint_as_float(gq[hf2].x) + __uint_as_float(gq[2 + hf2].x)) + (__uint_as_float(gq[4 + hf2].x) + __uint_as_float(gq[6 + hf2].x));
                    const float q1 = (__uint_as_float(gq[hf2].z) + __uint_as_float(gq[2 + hf2].z)) + (__uint_as_float(gq[4 + hf2].z) + __uint_as_float(gq[6 + hf2].z));
                    ((unsigned*)(lds + L_Q))[hf2 * 64 + lane] = cvtpk(q0, q1);
                }
                if (lane == 0) qflag[1] = T;
                asm volatile("s_waitcnt lgkmcnt(0)" ::: "memory");
                if (lane == 0) *qflag = 1u;
                STAMPW(12, true);
            }
            __builtin_amdgcn_s_setprio(0);
        } else {
            ep = __builtin_amdgcn_raw_buffer_load_b32(rws, WS_E, 0, 16);
            __syncthreads();
        }
        T = __builtin_amdgcn_readfirstlane(ep) + 1u;
        __builtin_amdgcn_sched_barrier(0);
        v4u Wt[12];
#pragma unroll
        for (int j = 0; j < 12; ++j) { const int u = sw + 4 * j; Wt[j] = LDNT(rt, (8 * u < trows) ? ((trows * trg + 8 * u + lrow) * D + 64 * tcg) * 2 + lcol : OOB); }
        __builtin_amdgcn_sched_barrier(0);
        STAMPW(13, sw == 3);
        if (role_w2) {
            const int st = tid - 512;
            const __amdgpu_buffer_rsrc_t rb1 = RSRC(b1, F * 2);
            v4u gh[2][3]; unsigned b1v[2]; int hoff[2];
#pragma unroll
            for (int e = 0; e < 2; ++e) {
                const int it = st + 256 * e; const bool valid = it < 384;
                const int hbb = it / 192, hpr = it % 192, hf = 384 * trg + 2 * hpr;
                hoff[e] = valid ? WS_HP + ((((hf >> 6) * 2 + hbb) * 3) * 32 + ((hf & 63) >> 1)) * 16 : OOB;
                b1v[e] = __builtin_amdgcn_raw_buffer_load_b32(rb1, valid ? hf * 2 : OOB, 0, 0);
            }
            bool ok; unsigned spin = 0;
            do {
                ok = true;
#pragma unroll
                for (int e = 0; e < 2; ++e)
#pragma unroll
                    for (int i = 0; i < 3; ++i) { gh[e][i] = LDSC(rws, hoff[e] == OOB ? OOB : hoff[e] + i * 32 * 16); }
#pragma unroll
                for (int e = 0; e < 2; ++e)
#pragma unroll
                    for (int i = 0; i < 3; ++i) ok &= (hoff[e] == OOB) | tag_ok(gh[e][i], T);
                if (__all(ok)) break;
                SPIN_PAUSE(2);
            } while (++spin < SPIN_CAP);
#pragma unroll
            for (int e = 0; e < 2; ++e) {
                const int it = st + 256 * e;
                float h0 = __uint_as_float(gh[e][0].x) + __uint_as_float(gh[e][1].x) + __uint_as_float(gh[e][2].x);
                float h1 = __uint_as_float(gh[e][0].z) + __uint_as_float(gh[e][1].z) + __uint_as_float(gh[e][2].z);
                h0 = rbf(rbf(h0) + bf2f(b1v[e] & 0xffffu)); h1 = rbf(rbf(h1) + __uint_as_float(b1v[e] & 0xffff0000u));
                h0 = h0 > 0.f ? h0 : 0.f; h1 = h1 > 0.f ? h1 : 0.f;
                if (it < 384) ((unsigned*)(lds + L_ACT))[it] = cvtpk(h0 * h0, h1 * h1);
            }
        }
        __syncthreads();
        __syncthreads();
        {
            const u16* act = (const u16*)(lds + L_ACT);
            f32x4 at[2] = {{0, 0, 0, 0}, {0, 0, 0, 0}};
#pragma unroll
            for (int j = 0; j < 12; ++j) {
                const int r = min(8 * (sw + 4 * j) + lrow, trows - 1);
#pragma unroll
                for (int bb = 0; bb < 2; ++bb) { const unsigned v = act[bb * 384 + r]; const unsigned dup = v | (v << 16); at[bb] = mfma(Wt[j], BDIAG(dup), at[bb]); }
            }
            float* red2 = (float*)(lds + L_RED2);
            if ((lane >> 5) == ((lane >> 3) & 1)) {
                const int r1 = lane >> 5, cb = 32 * ((lane >> 4) & 1) + (lane & 7);
#pragma unroll
                for (int bb = 0; bb < 2; ++bb)
#pragma unroll
                    for (int r = 0; r < 4; ++r) red2[((sw * 2 + r1) * 2 + bb) * 64 + cb + 8 * r] = at[bb][r];
            }
        }
        __syncthreads();
    }
    if ((role_wo || role_w2) && tid < 64) {
        const float* red2 = (const float*)(lds + L_RED2);
        const int bb = tid >> 5, pr = tid & 31;
        float v0 = 0.f, v1 = 0.f;
#pragma unroll
        for (int i = 0; i < 8; ++i) { const float2 t = *(const float2*)&red2[(i * 2 + bb) * 64 + 2 * pr]; v0 += t.x; v1 += t.y; }
        const int off = role_wo ? WS_SP + (((tcg * 2 + bb) * 16 + trg) * 32 + pr) * 16 : WS_MP + (((tcg * 2 + bb) * 12 + trg) * 32 + pr) * 16;
        STSC(mkpair(v0, v1, T), rws, off);
    }
    STAMPW(9, w == 0);

    if (bid >= 144 && bid < 216 && w == 1) {
        const int u = bid - 144, cg = u >> 3, fb = (u >> 2) & 1, q4 = u & 3;
        const int p8 = lane >> 3, j = lane & 7;
        const int col = 64 * cg + 16 * q4 + 2 * j;
        const unsigned xw = *(const unsigned*)(x + fb * D + col), bw = *(const unsigned*)(b2 + col);
        const int offs = WS_SP + (((cg * 2 + fb) * 16 + p8) * 32 + 8 * q4 + j) * 16;
        const int offm = WS_MP + (((cg * 2 + fb) * 12 + p8) * 32 + 8 * q4 + j) * 16;
        v4u g[4]; bool ok; unsigned spin = 0;
        do {
            g[0] = LDSC(rws, offs); g[1] = LDSC(rws, offs + 8 * 32 * 16); g[2] = LDSC(rws, offm); g[3] = LDSC(rws, p8 < 4 ? offm + 8 * 32 * 16 : OOB);
            ok = tag_ok(g[0], T) & tag_ok(g[1], T) & tag_ok(g[2], T) & ((p8 >= 4) | tag_ok(g[3], T));
            if (__all(ok)) break;
            SPIN_PAUSE(1);
        } while (++spin < SPIN_CAP);
        float s0 = __uint_as_float(g[0].x) + __uint_as_float(g[1].x), s1 = __uint_as_float(g[0].z) + __uint_as_float(g[1].z);
        float m0 = __uint_as_float(g[2].x) + __uint_as_float(g[3].x), m1 = __uint_as_float(g[2].z) + __uint_as_float(g[3].z);
        s0 += dpp<0x128>(s0); s1 += dpp<0x128>(s1); m0 += dpp<0x128>(m0); m1 += dpp<0x128>(m1);
        s0 = xrow_sum(s0); s1 = xrow_sum(s1); m0 = xrow_sum(m0); m1 = xrow_sum(m1);
        if (lane < 8) {
            const float mv0 = rbf(rbf(m0) + bf2f(bw & 0xffffu)), mv1 = rbf(rbf(m1) + __uint_as_float(bw & 0xffff0000u));
            const float g0 = rbf(mv0 / (1.f + __expf(-mv0))), g1 = rbf(mv1 / (1.f + __expf(-mv1)));
            *(unsigned*)(out + fb * D + col) = cvtpk(bf2f(xw & 0xffffu) + rbf(rbf(s0) * g0), __uint_as_float(xw & 0xffff0000u) + rbf(rbf(s1) * g1));
        }
        if (u == 0 && lane == 0) __builtin_amdgcn_raw_buffer_store_b32(T, rws, WS_E, 0, 16);
        STAMPW(14, true);
    }
    STAMPW(14, w == 0);
}

extern "C" void kernel_launch(void* const* d_in, const int* in_sizes, int n_in, void* d_out, int out_size, void* d_ws, size_t ws_size, hipStream_t stream) {
    fused_kernel<<<256, 768, 0, stream>>>((const u16*)d_in[0], (const u16*)d_in[1], (const u16*)d_in[2], (const u16*)d_in[3], (const u16*)d_in[4],
                                          (const u16*)d_in[6], (const u16*)d_in[7], (const u16*)d_in[8], (const u16*)d_in[9], (u16*)d_out, (unsigned char*)d_ws);
}
```

```cpp
#include <hip/hip_runtime.h>
#include <stdint.h>

typedef unsigned short u16;
typedef unsigned v4u __attribute__((ext_vector_type(4)));
typedef float f32x4 __attribute__((ext_vector_type(4)));
typedef short bf16x8 __attribute__((ext_vector_type(8)));
typedef __bf16 bf16x2 __attribute__((ext_vector_type(2)));
#define LAS __attribute__((address_space(3)))
#define STAG 14
#define NFIN 1
#define HSLEEP 2
#define B0MODE 0

constexpr int D = 576, HKV = 16, SK = 16384, DH = 128, WIN = 4096, F = 4608;
constexpr int WS_E = 0;
constexpr int WS_QP = 4096;
constexpr int WS_HP = WS_QP + 16 * 2 * 4 * 128 * 16;
constexpr int WS_AP = WS_HP + 72 * 2 * 3 * 32 * 16;
constexpr int AP_REC = 136 * 16;
constexpr int WS_SP = WS_AP + 256 * AP_REC;
constexpr int WS_MP = WS_SP + 9 * 2 * 16 * 32 * 16;
constexpr int WS_END = WS_MP + 9 * 2 * 12 * 32 * 16;
constexpr int WS_STAMP = (WS_END + 4095) / 4096 * 4096;
constexpr unsigned SPIN_CAP = 1u << 18;
constexpr int OOB = 0x7ffffff0;

#define RSRC(p, n) __builtin_amdgcn_make_buffer_rsrc((void*)(p), 0, (n), 0x00020000)
#define LDNT(rs, off) __builtin_amdgcn_raw_buffer_load_b128(rs, off, 0, 2)
#define LDSC(rs, off) __builtin_amdgcn_raw_buffer_load_b128(rs, off, 0, 16)
#define STSC(v, rs, off) __builtin_amdgcn_raw_buffer_store_b128(v, rs, off, 0, 16)

__device__ __forceinline__ float bf2f(unsigned b) { return __uint_as_float(b << 16); }
typedef float f32x2 __attribute__((ext_vector_type(2)));
__device__ __forceinline__ unsigned cvtpk(float lo, float hi) { const f32x2 v = {lo, hi}; return __builtin_bit_cast(unsigned, __builtin_convertvector(v, bf16x2)); }
__device__ __forceinline__ float rbf(float f) { return __uint_as_float(cvtpk(f, f) << 16); }
__device__ __forceinline__ float dot2(unsigned k, unsigned q, float acc) {
    return __builtin_amdgcn_fdot2_f32_bf16(__builtin_bit_cast(bf16x2, k), __builtin_bit_cast(bf16x2, q), acc, false);
}
template <int CTRL> __device__ __forceinline__ float dpp(float x) {
    return __builtin_bit_cast(float, __builtin_amdgcn_mov_dpp(__builtin_bit_cast(int, x), CTRL, 0xf, 0xf, true));
}
__device__ __forceinline__ f32x4 mfma(v4u a, v4u b, f32x4 c) {
    return __builtin_amdgcn_mfma_f32_16x16x32_bf16(__builtin_bit_cast(bf16x8, a), __builtin_bit_cast(bf16x8, b), c, 0, 0, 0);
}
__device__ __forceinline__ float xrow_sum(float x) {
    auto s = __builtin_amdgcn_permlane16_swap(__float_as_uint(x), __float_as_uint(x), false, false);
    x = __uint_as_float(s[0]) + __uint_as_float(s[1]);
    auto t = __builtin_amdgcn_permlane32_swap(__float_as_uint(x), __float_as_uint(x), false, false);
    return __uint_as_float(t[0]) + __uint_as_float(t[1]);
}
__device__ __forceinline__ float xrow_max(float x) {
    auto s = __builtin_amdgcn_permlane16_swap(__float_as_uint(x), __float_as_uint(x), false, false);
    x = fmaxf(__uint_as_float(s[0]), __uint_as_float(s[1]));
    auto t = __builtin_amdgcn_permlane32_swap(__float_as_uint(x), __float_as_uint(x), false, false);
    return fmaxf(__uint_as_float(t[0]), __uint_as_float(t[1]));
}
__device__ __forceinline__ float wave_sum(float v) {
    v += dpp<0xB1>(v); v += dpp<0x4E>(v); v += dpp<0x141>(v); v += dpp<0x140>(v);
    return xrow_sum(v);
}
__device__ __forceinline__ bool tag_ok(v4u p, unsigned T) { return (p.y == T) & (p.w == ~T); }
__device__ __forceinline__ v4u mkpair(float a, float b, unsigned T) { return v4u{__float_as_uint(a), T, __float_as_uint(b), ~T}; }

#define SPIN_PAUSE(n) do { __builtin_amdgcn_s_sleep(n); asm volatile("" :: "s"(ws) : "memory"); } while (0)
#define STAMPW(i, cond) do { } while (0)

constexpr int L_XS = 0;
constexpr int L_Q = 4608;
constexpr int L_ACT = 13312 + 8192;
constexpr int L_ML = 6656;
constexpr int L_GML = 6784;
constexpr int L_FLAG = 7040;
constexpr int L_SCR = 7168;
constexpr int L_RED2 = 9216;
constexpr int L_RED = 13312;
constexpr int L_K = L_RED + 16384;
constexpr int L_BYTES = L_K + 131072;

__global__ void __launch_bounds__(768, 3)
fused_kernel(const u16* __restrict__ x, const u16* __restrict__ kc, const u16* __restrict__ vc, const u16* __restrict__ wq, const u16* __restrict__ wo,
             const u16* __restrict__ w1, const u16* __restrict__ b1, const u16* __restrict__ w2, const u16* __restrict__ b2,
             u16* __restrict__ out, unsigned char* ws) {
    __shared__ __attribute__((aligned(16))) unsigned char lds[L_BYTES];
    const int tid = threadIdx.x, lane = tid & 63, w = __builtin_amdgcn_readfirstlane(tid >> 6), bid = blockIdx.x;
    STAMPW(0, w == 0);
    const int s = bid & 7, b = (bid >> 3) & 1, kvh = bid >> 4;
    const int kq = (bid & 15) >> 2, cq = bid & 3;
    const bool has_w1 = bid < 216;
    const int c1 = bid % 72, k1 = bid / 72;
    const bool role_wo = bid < 144, role_w2 = bid >= 144 && bid < 252;
    const int u2 = bid - 144;
    const int tcg = role_wo ? bid % 9 : u2 % 9;
    const int trg = role_wo ? bid / 9 : u2 / 9;
    const int trows = role_wo ? 256 : 384;
    const __amdgpu_buffer_rsrc_t rws = RSRC(ws, WS_END);
    volatile LAS unsigned* qflag = (volatile LAS unsigned*)(LAS unsigned char*)(lds + L_FLAG);
    if (tid < 8) qflag[tid] = 0u;

    const int lrow = lane >> 3, lcol = (lane & 7) * 16;
    const unsigned hm = (lane & 1) ? 0xffff0000u : 0x0000ffffu;
    const int dsel = (lane >> 1) & 3;
    const unsigned M0 = dsel == 0 ? hm : 0u, M1 = dsel == 1 ? hm : 0u, M2 = dsel == 2 ? hm : 0u, M3 = dsel == 3 ? hm : 0u;
#define BDIAG(dup) v4u{(dup) & M0, (dup) & M1, (dup) & M2, (dup) & M3}
    unsigned T;

    if (w < 8) {
        const __amdgpu_buffer_rsrc_t rk = RSRC(kc + ((size_t)(b * HKV + kvh) * SK + (SK - WIN) + s * 512 + w * 64) * DH, 64 * 256);
        const __amdgpu_buffer_rsrc_t rv = RSRC(vc + ((size_t)(b * HKV + kvh) * SK + (SK - WIN) + s * 512 + w * 64) * DH, 64 * 256);
        v4u K[16], V[16];
        __syncthreads();
        __builtin_amdgcn_s_setprio(1);
#pragma unroll
        for (int j = 0; j < 16; ++j) K[j] = LDNT(rk, j * 1024 + lane * 16);
        __builtin_amdgcn_sched_barrier(0);
        __builtin_amdgcn_s_setprio(0);
        __builtin_amdgcn_sched_barrier(0);
#pragma unroll
        for (int j = 0; j < 16; ++j) V[j] = LDNT(rv, j * 1024 + lane * 16);
        __builtin_amdgcn_sched_barrier(0);
        if (lane == 0) __hip_atomic_fetch_add((LAS unsigned*)(qflag + 5), 1u, __ATOMIC_RELAXED, __HIP_MEMORY_SCOPE_WORKGROUP);
        STAMPW(1, w == 0); STAMPW(2, w == 7);
        const int g = lane >> 4, c = lane & 15;
#pragma unroll
        for (int j = 0; j < 16; ++j) { const int i = 4 * (j & 3) + g; *(v4u*)(lds + L_K + w * 16384 + (j >> 2) * 4096 + i * 256 + ((c ^ (4 * g + (j & 3))) * 16)) = K[j]; }
        { unsigned spin = 0; while (*qflag == 0u && ++spin < (SPIN_CAP << 4)) __builtin_amdgcn_s_sleep(1); T = qflag[1]; }
        asm volatile("s_waitcnt lgkmcnt(0)" ::: "memory");
        STAMPW(3, w == 0); STAMPW(4, w == 7);
        v4u qf[4];
#pragma unroll
        for (int ks = 0; ks < 4; ++ks) qf[ks] = *(const v4u*)(lds + L_Q + (c >> 3) * 256 + (4 * ks + g) * 16);
        f32x4 sc[4];
#pragma unroll
        for (int t = 0; t < 4; ++t) {
            sc[t] = f32x4{0, 0, 0, 0};
#pragma unroll
            for (int ks = 0; ks < 4; ++ks) {
                const v4u ka = *(const v4u*)(lds + L_K + w * 16384 + t * 4096 + (4 * (c & 3) + (c >> 2)) * 256 + (((4 * ks + g) ^ c) * 16));
                sc[t] = mfma(ka, qf[ks], sc[t]);
            }
        }
        float m = -INFINITY;
#pragma unroll
        for (int t = 0; t < 4; ++t)
#pragma unroll
            for (int r = 0; r < 4; ++r) { sc[t][r] = rbf(rbf(sc[t][r]) * 0.088388347648318440f); m = fmaxf(m, sc[t][r]); }
        m = xrow_max(m);
        float l = 0.f;
        constexpr float LOG2E = 1.4426950408889634f;
        const float mm = m * LOG2E;
        unsigned pd[16];
#pragma unroll
        for (int t = 0; t < 4; ++t)
#pragma unroll
            for (int r = 0; r < 4; ++r) { const float p = __builtin_amdgcn_exp2f(sc[t][r] * LOG2E - mm); l += p; pd[4 * t + r] = cvtpk(p, p); }
        l = xrow_sum(l);
        STAMPW(5, w == 7);
        f32x4 acc0 = {0, 0, 0, 0}, acc1 = {0, 0, 0, 0};
#pragma unroll
        for (int t = 0; t < 16; t += 2) { acc0 = mfma(V[t], BDIAG(pd[t]), acc0); acc1 = mfma(V[t + 1], BDIAG(pd[t + 1]), acc1); }
        acc0 += acc1;
        __builtin_amdgcn_s_setprio(3);
        const int hh = (lane >> 3) & 1;
        *(f32x4*)(lds + L_RED + (w * 64 + lane) * 16) = acc0;
        if ((lane & 0x37) == 0) *(float2*)(lds + L_ML + (w * 2 + hh) * 8) = make_float2(mm, l);
        asm volatile("s_waitcnt lgkmcnt(0)" ::: "memory");
        unsigned arr = 0;
        if (lane == 0) arr = __hip_atomic_fetch_add((LAS unsigned*)(qflag + 2), 1u, __ATOMIC_RELAXED, __HIP_MEMORY_SCOPE_WORKGROUP);
        arr = __builtin_amdgcn_readfirstlane(arr);
        asm volatile("" ::: "memory");
        if (arr == 7u) {
            float2 ml[8]; f32x4 pa[8];
#pragma unroll
            for (int i = 0; i < 8; ++i) { ml[i] = *(const float2*)(lds + L_ML + (i * 2 + hh) * 8); pa[i] = *(const f32x4*)(lds + L_RED + (i * 64 + lane) * 16); }
            float M = ml[0].x;
#pragma unroll
            for (int i = 1; i < 8; ++i) M = fmaxf(M, ml[i].x);
            f32x4 a = {0, 0, 0, 0}; float L = 0.f;
#pragma unroll
            for (int i = 0; i < 8; ++i) { const float wt = __builtin_amdgcn_exp2f(ml[i].x - M); a += wt * pa[i]; L += wt * ml[i].y; }
            const int off = WS_AP + bid * AP_REC + lane * 16;
            STSC(mkpair(a[0], a[1], T), rws, off);
            STSC(mkpair(a[2], a[3], T), rws, off + 1024);
            STSC(mkpair(M, L, T), rws, (lane & 0x37) == 0 ? WS_AP + bid * AP_REC + (128 + hh) * 16 : OOB);
            if (lane == 0) qflag[6] = 1u;
        }
        if (role_wo && w < 4) {
            { unsigned spin = 0; while (qflag[6] == 0u && ++spin < (SPIN_CAP << 4)) __builtin_amdgcn_s_sleep(2); }
            const int bb = (w >> 1) & 1, h2 = w & 1;
            const unsigned bit = 1u << (w & 3);
            const int base = WS_AP + (trg * 16 + bb * 8) * AP_REC;
            v4u g[8], gm[8];
            bool ok = false;
            unsigned spin = 0;
            do {
                if (qflag[3] & bit) break;
#pragma unroll
                for (int i = 0; i < 8; ++i) { g[i] = LDSC(rws, base + i * AP_REC + ((lane >> 5) * 64 + ((lane >> 3) & 3) * 16 + h2 * 8 + (lane & 7)) * 16); gm[i] = LDSC(rws, base + i * AP_REC + (128 + h2) * 16); }
                bool o = true;
#pragma unroll
                for (int i = 0; i < 8; ++i) o &= tag_ok(g[i], T) & tag_ok(gm[i], T);
                ok = __all(o);
                if (ok) break;
                SPIN_PAUSE(HSLEEP);
            } while (++spin < SPIN_CAP);
            if (ok) {
                float M = __uint_as_float(gm[0].x);
#pragma unroll
                for (int i = 1; i < 8; ++i) M = fmaxf(M, __uint_as_float(gm[i].x));
                float a0 = 0.f, a1 = 0.f, L = 0.f;
#pragma unroll
                for (int i = 0; i < 8; ++i) {
                    const float wt = __builtin_amdgcn_exp2f(__uint_as_float(gm[i].x) - M);
                    a0 += wt * __uint_as_float(g[i].x); a1 += wt * __uint_as_float(g[i].z); L += wt * __uint_as_float(gm[i].z);
                }
                const float ri = 1.f / L;
                const int dl = h2 * 128 + 32 * ((lane >> 3) & 3) + 16 * (lane >> 5) + (lane & 7);
                unsigned* act = (unsigned*)(lds + L_ACT);
                act[dl * 2 + bb] = cvtpk(a0 * ri, a0 * ri); act[(dl + 8) * 2 + bb] = cvtpk(a1 * ri, a1 * ri);
                asm volatile("s_waitcnt lgkmcnt(0)" ::: "memory");
                if (lane == 0) __hip_atomic_fetch_or((LAS unsigned*)(qflag + 3), bit, __ATOMIC_RELAXED, __HIP_MEMORY_SCOPE_WORKGROUP);
            } else if (spin >= SPIN_CAP) {
                if (lane == 0) __hip_atomic_fetch_or((LAS unsigned*)(qflag + 3), bit, __ATOMIC_RELAXED, __HIP_MEMORY_SCOPE_WORKGROUP);
            }
        }
    } else {
        const int sw = w - 8;
        const __amdgpu_buffer_rsrc_t rt = RSRC(role_wo ? wo : w2, role_wo ? 4096 * D * 2 : (role_w2 ? F * D * 2 : 0));
        if (sw < 2) {
            __builtin_amdgcn_s_setprio(3);
            const __amdgpu_buffer_rsrc_t rx = RSRC(x, 2 * D * 2);
            const __amdgpu_buffer_rsrc_t rw = sw == 0 ? RSRC(wq + (size_t)(144 * kq) * 4096 + 256 * kvh + 64 * cq, 144 * 4096 * 2 - 256 * kvh * 2 - 128 * cq)
                                                      : RSRC(w1 + (size_t)(192 * k1) * F + 64 * c1, has_w1 ? 192 * F * 2 - 128 * c1 : 0);
            const int ld = sw == 0 ? 4096 * 2 : F * 2;
            const int row0 = sw == 0 ? 144 * kq : 192 * k1;
            const int nld = sw == 0 ? 18 : 24;
            v4u xr[3];
#pragma unroll
            for (int j = 0; j < 3; ++j) xr[j] = __builtin_amdgcn_raw_buffer_load_b128(rx, (lane + 64 * j) * 16, 0, 0);
            v4u Wf[24];
#pragma unroll
            for (int j = 0; j < 24; ++j) Wf[j] = LDNT(rw, j < nld ? (8 * j + lrow) * ld + lcol : OOB);
            __builtin_amdgcn_sched_barrier(0);
            __syncthreads();
            __builtin_amdgcn_sched_barrier(0);
            unsigned char* xsb = lds + L_XS + sw * 2304;
            {
                float xv[3][8];
#pragma unroll
                for (int j = 0; j < 3; ++j)
#pragma unroll
                    for (int k = 0; k < 4; ++k) { xv[j][2 * k] = bf2f(xr[j][k] & 0xffffu); xv[j][2 * k + 1] = __uint_as_float(xr[j][k] & 0xffff0000u); }
                float sj[3];
#pragma unroll
                for (int j = 0; j < 3; ++j) { float t = 0.f;
#pragma unroll
                    for (int k = 0; k < 8; ++k) t += xv[j][k];
                    sj[j] = t; }
                const bool c1b0 = lane < 8, c2ok = lane < 16;
                const float mean0 = wave_sum(sj[0] + (c1b0 ? sj[1] : 0.f)) * (1.f / D);
                const float mean1 = wave_sum((c1b0 ? 0.f : sj[1]) + sj[2]) * (1.f / D);
                const float mj[3] = {mean0, c1b0 ? mean0 : mean1, mean1};
#pragma unroll
                for (int j = 0; j < 3; ++j) { float t = 0.f;
#pragma unroll
                    for (int k = 0; k < 8; ++k) { const float d = xv[j][k] - mj[j]; t += d * d; }
                    sj[j] = t; }
                if (!c2ok) sj[2] = 0.f;
                const float var0 = wave_sum(sj[0] + (c1b0 ? sj[1] : 0.f)) * (1.f / D);
                const float var1 = wave_sum((c1b0 ? 0.f : sj[1]) + sj[2]) * (1.f / D);
                const float mb0 = rbf(mean0), mb1 = rbf(mean1);
                const float den0 = rbf(sqrtf(rbf(rbf(var0) + 1e-5f))), den1 = rbf(sqrtf(rbf(rbf(var1) + 1e-5f)));
                const float ri0 = 1.f / den0, ri1 = 1.f / den1;
                const float mbj[3] = {mb0, c1b0 ? mb0 : mb1, mb1}, dnj[3] = {ri0, c1b0 ? ri0 : ri1, ri1};
#pragma unroll
                for (int j = 0; j < 3; ++j) {
                    v4u o;
#pragma unroll
                    for (int k = 0; k < 4; ++k) o[k] = cvtpk(rbf(xv[j][2 * k] - mbj[j]) * dnj[j], rbf(xv[j][2 * k + 1] - mbj[j]) * dnj[j]);
                    if (j < 2 || c2ok) *(v4u*)(xsb + (lane + 64 * j) * 16) = o;
                }
            }
            __builtin_amdgcn_fence(__ATOMIC_ACQ_REL, "wavefront");
            asm volatile("" ::: "memory");
            STAMPW(10, sw == 0);
            T = *(const __attribute__((address_space(4))) unsigned*)(uintptr_t)(ws + WS_E) + 1u; asm volatile("" : "+s"(T));
            const u16* xs = (const u16*)xsb;
            f32x4 af[2] = {{0, 0, 0, 0}, {0, 0, 0, 0}};
#pragma unroll
            for (int j = 0; j < 24; ++j) {
                const int r = min(row0 + 8 * j + lrow, D - 1);
#pragma unroll
                for (int bb = 0; bb < 2; ++bb) { const unsigned v = xs[bb * D + r]; const unsigned dup = v | (v << 16); af[bb] = mfma(Wf[j], BDIAG(dup), af[bb]); }
            }
            float* scr = (float*)(lds + L_SCR + sw * 1024);
            if ((lane >> 5) == ((lane >> 3) & 1)) {
                const int r1 = lane >> 5, cb = 32 * ((lane >> 4) & 1) + (lane & 7);
#pragma unroll
                for (int bb = 0; bb < 2; ++bb)
#pragma unroll
                    for (int r = 0; r < 4; ++r) scr[(r1 * 2 + bb) * 64 + cb + 8 * r] = af[bb][r];
            }
            __builtin_amdgcn_fence(__ATOMIC_ACQ_REL, "wavefront");
            asm volatile("" ::: "memory");
            {
                const int bb = lane >> 5, pr = lane & 31;
                const float2 t0 = *(const float2*)&scr[bb * 64 + 2 * pr], t1 = *(const float2*)&scr[(2 + bb) * 64 + 2 * pr];
                const int off = sw == 0 ? WS_QP + (((kvh * 2 + bb) * 4 + kq) * 128 + cq * 32 + pr) * 16
                                        : (has_w1 ? WS_HP + (((c1 * 2 + bb) * 3 + k1) * 32 + pr) * 16 : OOB);
                STSC(mkpair(t0.x + t1.x, t0.y + t1.y, T), rws, off);
            }
            STAMPW(11, sw == 0);
            if (sw == 0) {
                const int qoff = WS_QP + ((kvh * 2 + b) * 4 * 128 + lane) * 16;
                v4u gq[8]; bool ok; unsigned spin = 0;
                do {
#pragma unroll
                    for (int i = 0; i < 8; ++i) gq[i] = LDSC(rws, qoff + (i >> 1) * 128 * 16 + (i & 1) * 64 * 16);
                    ok = true;
#pragma unroll
                    for (int i = 0; i < 8; ++i) ok &= tag_ok(gq[i], T);
                    if (__all(ok)) break;
                    SPIN_PAUSE(1);
                } while (++spin < SPIN_CAP);
#pragma unroll
                for (int hf2 = 0; hf2 < 2; ++hf2) {
                    const float q0 = (__uint_as_float(gq[hf2].x) + __uint_as_float(gq[2 + hf2].x)) + (__uint_as_float(gq[4 + hf2].x) + __uint_as_float(gq[6 + hf2].x));
                    const float q1 = (__uint_as_float(gq[hf2].z) + __uint_as_float(gq[2 + hf2].z)) + (__uint_as_float(gq[4 + hf2].z) + __uint_as_float(gq[6 + hf2].z));
                    ((unsigned*)(lds + L_Q))[hf2 * 64 + lane] = cvtpk(q0, q1);
                }
                if (lane == 0) qflag[1] = T;
                asm volatile("s_waitcnt lgkmcnt(0)" ::: "memory");
                if (lane == 0) *qflag = 1u;
                STAMPW(12, true);
            }
            __builtin_amdgcn_s_setprio(0);
        } else {
            __syncthreads();
        }
        T = *(const __attribute__((address_space(4))) unsigned*)(uintptr_t)(ws + WS_E) + 1u; asm volatile("" : "+s"(T));
        __builtin_amdgcn_sched_barrier(0);
        { unsigned spin = 0; while (qflag[5] < 8u && ++spin < (SPIN_CAP << 4)) __builtin_amdgcn_s_sleep(2); }
        v4u Wt[12];
#pragma unroll
        for (int j = 0; j < 12; ++j) { const int u = sw + 4 * j; Wt[j] = LDNT(rt, (8 * u < trows) ? ((trows * trg + 8 * u + lrow) * D + 64 * tcg) * 2 + lcol : OOB); }
        __builtin_amdgcn_sched_barrier(0);
        STAMPW(13, sw == 3);
        if (role_w2) {
            const int st = tid - 512;
            const __amdgpu_buffer_rsrc_t rb1 = RSRC(b1, F * 2);
            v4u gh[2][3]; unsigned b1v[2]; int hoff[2];
#pragma unroll
            for (int e = 0; e < 2; ++e) {
                const int it = st + 256 * e; const bool valid = it < 384;
                const int hbb = it / 192, hpr = it % 192, hf = 384 * trg + 2 * hpr;
                hoff[e] = valid ? WS_HP + ((((hf >> 6) * 2 + hbb) * 3) * 32 + ((hf & 63) >> 1)) * 16 : OOB;
                b1v[e] = __builtin_amdgcn_raw_buffer_load_b32(rb1, valid ? hf * 2 : OOB, 0, 0);
            }
            bool ok; unsigned spin = 0;
            do {
                ok = true;
#pragma unroll
                for (int e = 0; e < 2; ++e)
#pragma unroll
                    for (int i = 0; i < 3; ++i) { gh[e][i] = LDSC(rws, hoff[e] == OOB ? OOB : hoff[e] + i * 32 * 16); }
#pragma unroll
                for (int e = 0; e < 2; ++e)
#pragma unroll
                    for (int i = 0; i < 3; ++i) ok &= (hoff[e] == OOB) | tag_ok(gh[e][i], T);
                if (__all(ok)) break;
                SPIN_PAUSE(2);
            } while (++spin < SPIN_CAP);
#pragma unroll
            for (int e = 0; e < 2; ++e) {
                const int it = st + 256 * e;
                float h0 = __uint_as_float(gh[e][0].x) + __uint_as_float(gh[e][1].x) + __uint_as_float(gh[e][2].x);
                float h1 = __uint_as_float(gh[e][0].z) + __uint_as_float(gh[e][1].z) + __uint_as_float(gh[e][2].z);
                h0 = rbf(rbf(h0) + bf2f(b1v[e] & 0xffffu)); h1 = rbf(rbf(h1) + __uint_as_float(b1v[e] & 0xffff0000u));
                h0 = h0 > 0.f ? h0 : 0.f; h1 = h1 > 0.f ? h1 : 0.f;
                h0 *= h0; h1 *= h1;
                const int hbb = it / 192, hpr = it % 192;
                if (it < 384) { unsigned* act = (unsigned*)(lds + L_ACT); act[(2 * hpr) * 2 + hbb] = cvtpk(h0, h0); act[(2 * hpr + 1) * 2 + hbb] = cvtpk(h1, h1); }
            }
            asm volatile("s_waitcnt lgkmcnt(0)" ::: "memory");
            if (lane == 0) __hip_atomic_fetch_or((LAS unsigned*)(qflag + 3), 1u << sw, __ATOMIC_RELAXED, __HIP_MEMORY_SCOPE_WORKGROUP);
        }
        if (role_wo || role_w2) {
            { unsigned spin = 0; while (qflag[3] != 15u && ++spin < (SPIN_CAP << 4)) __builtin_amdgcn_s_sleep(0); }
            asm volatile("" ::: "memory");
            const unsigned* act = (const unsigned*)(lds + L_ACT);
            f32x4 at[2][2] = {{{0, 0, 0, 0}, {0, 0, 0, 0}}, {{0, 0, 0, 0}, {0, 0, 0, 0}}};
            typedef unsigned v2u __attribute__((ext_vector_type(2)));
            v2u ad[8];
#pragma unroll
            for (int j = 0; j < 8; ++j) ad[j] = *(const v2u*)&act[(8 * (sw + 4 * j) + lrow) * 2];
#pragma unroll
            for (int j = 0; j < 8; ++j) { at[0][j & 1] = mfma(Wt[j], BDIAG(ad[j].x), at[0][j & 1]); at[1][j & 1] = mfma(Wt[j], BDIAG(ad[j].y), at[1][j & 1]); }
            if (role_w2) {
#pragma unroll
                for (int j = 8; j < 12; ++j) { const v2u a2 = *(const v2u*)&act[(8 * (sw + 4 * j) + lrow) * 2]; at[0][j & 1] = mfma(Wt[j], BDIAG(a2.x), at[0][j & 1]); at[1][j & 1] = mfma(Wt[j], BDIAG(a2.y), at[1][j & 1]); }
            }
            at[0][0] += at[0][1]; at[1][0] += at[1][1];
            float* red2 = (float*)(lds + L_RED2);
            if ((lane >> 5) == ((lane >> 3) & 1)) {
                const int r1 = lane >> 5, cb = 32 * ((lane >> 4) & 1) + (lane & 7);
#pragma unroll
                for (int bb = 0; bb < 2; ++bb)
#pragma unroll
                    for (int r = 0; r < 4; ++r) red2[((sw * 2 + r1) * 2 + bb) * 64 + cb + 8 * r] = at[bb][0][r];
            }
            asm volatile("s_waitcnt lgkmcnt(0)" ::: "memory");
            unsigned arr = 0;
            if (lane == 0) arr = __hip_atomic_fetch_add((LAS unsigned*)(qflag + 4), 1u, __ATOMIC_RELAXED, __HIP_MEMORY_SCOPE_WORKGROUP);
            arr = __builtin_amdgcn_readfirstlane(arr);
            asm volatile("" ::: "memory");
            if (arr == 3u) {
                const int bb = lane >> 5, pr = lane & 31;
                float v0 = 0.f, v1 = 0.f;
#pragma unroll
                for (int i = 0; i < 8; ++i) { const float2 t = *(const float2*)&red2[(i * 2 + bb) * 64 + 2 * pr]; v0 += t.x; v1 += t.y; }
                const int off = role_wo ? WS_SP + (((tcg * 2 + bb) * 16 + trg) * 32 + pr) * 16 : WS_MP + (((tcg * 2 + bb) * 12 + trg) * 32 + pr) * 16;
                STSC(mkpair(v0, v1, T), rws, off);
            }
        }
    }
    STAMPW(9, w == 0);

    if (bid >= 144 && bid < 216 && w >= 1 && w <= NFIN) {
        if (w == 2) __builtin_amdgcn_s_sleep(STAG); if (w == 3) __builtin_amdgcn_s_sleep(STAG / 2);
        const int u = bid - 144, cg = u >> 3, fb = (u >> 2) & 1, q4 = u & 3;
        const int p8 = lane >> 3, j = lane & 7;
        const int col = 64 * cg + 16 * q4 + 2 * j;
        const unsigned xw = *(const unsigned*)(x + fb * D + col), bw = *(const unsigned*)(b2 + col);
        const int offs = WS_SP + (((cg * 2 + fb) * 16 + p8) * 32 + 8 * q4 + j) * 16;
        const int offm = WS_MP + (((cg * 2 + fb) * 12 + p8) * 32 + 8 * q4 + j) * 16;
        v4u g[4]; bool ok; unsigned spin = 0;
        do {
            g[0] = LDSC(rws, offs); g[1] = LDSC(rws, offs + 8 * 32 * 16); g[2] = LDSC(rws, offm); g[3] = LDSC(rws, p8 < 4 ? offm + 8 * 32 * 16 : OOB);
            ok = tag_ok(g[0], T) & tag_ok(g[1], T) & tag_ok(g[2], T) & ((p8 >= 4) | tag_ok(g[3], T));
            if (__all(ok)) break;
            SPIN_PAUSE(1);
        } while (++spin < SPIN_CAP);
        float s0 = __uint_as_float(g[0].x) + __uint_as_float(g[1].x), s1 = __uint_as_float(g[0].z) + __uint_as_float(g[1].z);
        float m0 = __uint_as_float(g[2].x) + __uint_as_float(g[3].x), m1 = __uint_as_float(g[2].z) + __uint_as_float(g[3].z);
        s0 += dpp<0x128>(s0); s1 += dpp<0x128>(s1); m0 += dpp<0x128>(m0); m1 += dpp<0x128>(m1);
        s0 = xrow_sum(s0); s1 = xrow_sum(s1); m0 = xrow_sum(m0); m1 = xrow_sum(m1);
        if (lane < 8) {
            const float mv0 = rbf(rbf(m0) + bf2f(bw & 0xffffu)), mv1 = rbf(rbf(m1) + __uint_as_float(bw & 0xffff0000u));
            const float g0 = rbf(mv0 / (1.f + __expf(-mv0))), g1 = rbf(mv1 / (1.f + __expf(-mv1)));
            *(unsigned*)(out + fb * D + col) = cvtpk(bf2f(xw & 0xffffu) + rbf(rbf(s0) * g0), __uint_as_float(xw & 0xffff0000u) + rbf(rbf(s1) * g1));
        }
        if (u == 0 && lane == 0 && w == 1) __builtin_amdgcn_raw_buffer_store_b32(T, rws, WS_E, 0, 16);
        STAMPW(14, true);
    }
    STAMPW(14, w == 0);
}

extern "C" void kernel_launch(void* const* d_in, const int* in_sizes, int n_in, void* d_out, int out_size, void* d_ws, size_t ws_size, hipStream_t stream) {
    fused_kernel<<<256, 768, 0, stream>>>((const u16*)d_in[0], (const u16*)d_in[1], (const u16*)d_in[2], (const u16*)d_in[3], (const u16*)d_in[4],
                                          (const u16*)d_in[6], (const u16*)d_in[7], (const u16*)d_in[8], (const u16*)d_in[9], (u16*)d_out, (unsigned char*)d_ws);
}
```

```cpp
#include <hip/hip_runtime.h>
#include <stdint.h>

typedef unsigned short u16;
typedef unsigned v4u __attribute__((ext_vector_type(4)));
typedef float f32x4 __attribute__((ext_vector_type(4)));
typedef short bf16x8 __attribute__((ext_vector_type(8)));
typedef __bf16 bf16x2 __attribute__((ext_vector_type(2)));
#define LAS __attribute__((address_space(3)))
#define STAG 14
#define NFIN 1
#define HSLEEP 2
#define CODEPF 16
#define B0MODE 0

constexpr int D = 576, HKV = 16, SK = 16384, DH = 128, WIN = 4096, F = 4608;
constexpr int WS_E = 0;
constexpr int WS_QP = 4096;
constexpr int WS_HP = WS_QP + 16 * 2 * 4 * 128 * 16;
constexpr int WS_AP = WS_HP + 72 * 2 * 3 * 32 * 16;
constexpr int AP_REC = 136 * 16;
constexpr int WS_SP = WS_AP + 256 * AP_REC;
constexpr int WS_MP = WS_SP + 9 * 2 * 16 * 32 * 16;
constexpr int WS_END = WS_MP + 9 * 2 * 12 * 32 * 16;
constexpr int WS_STAMP = (WS_END + 4095) / 4096 * 4096;
constexpr unsigned SPIN_CAP = 1u << 18;
constexpr int OOB = 0x7ffffff0;

#define RSRC(p, n) __builtin_amdgcn_make_buffer_rsrc((void*)(p), 0, (n), 0x00020000)
#define LDNT(rs, off) __builtin_amdgcn_raw_buffer_load_b128(rs, off, 0, 2)
#define LDSC(rs, off) __builtin_amdgcn_raw_buffer_load_b128(rs, off, 0, 16)
#define STSC(v, rs, off) __builtin_amdgcn_raw_buffer_store_b128(v, rs, off, 0, 16)

__device__ __forceinline__ float bf2f(unsigned b) { return __uint_as_float(b << 16); }
typedef float f32x2 __attribute__((ext_vector_type(2)));
__device__ __forceinline__ unsigned cvtpk(float lo, float hi) { const f32x2 v = {lo, hi}; return __builtin_bit_cast(unsigned, __builtin_convertvector(v, bf16x2)); }
__device__ __forceinline__ float rbf(float f) { return __uint_as_float(cvtpk(f, f) << 16); }
__device__ __forceinline__ float dot2(unsigned k, unsigned q, float acc) {
    return __builtin_amdgcn_fdot2_f32_bf16(__builtin_bit_cast(bf16x2, k), __builtin_bit_cast(bf16x2, q), acc, false);
}
template <int CTRL> __device__ __forceinline__ float dpp(float x) {
    return __builtin_bit_cast(float, __builtin_amdgcn_mov_dpp(__builtin_bit_cast(int, x), CTRL, 0xf, 0xf, true));
}
__device__ __forceinline__ f32x4 mfma(v4u a, v4u b, f32x4 c) {
    return __builtin_amdgcn_mfma_f32_16x16x32_bf16(__builtin_bit_cast(bf16x8, a), __builtin_bit_cast(bf16x8, b), c, 0, 0, 0);
}
__device__ __forceinline__ float xrow_sum(float x) {
    auto s = __builtin_amdgcn_permlane16_swap(__float_as_uint(x), __float_as_uint(x), false, false);
    x = __uint_as_float(s[0]) + __uint_as_float(s[1]);
    auto t = __builtin_amdgcn_permlane32_swap(__float_as_uint(x), __float_as_uint(x), false, false);
    return __uint_as_float(t[0]) + __uint_as_float(t[1]);
}
__device__ __forceinline__ float xrow_max(float x) {
    auto s = __builtin_amdgcn_permlane16_swap(__float_as_uint(x), __float_as_uint(x), false, false);
    x = fmaxf(__uint_as_float(s[0]), __uint_as_float(s[1]));
    auto t = __builtin_amdgcn_permlane32_swap(__float_as_uint(x), __float_as_uint(x), false, false);
    return fmaxf(__uint_as_float(t[0]), __uint_as_float(t[1]));
}
__device__ __forceinline__ float wave_sum(float v) {
    v += dpp<0xB1>(v); v += dpp<0x4E>(v); v += dpp<0x141>(v); v += dpp<0x140>(v);
    return xrow_sum(v);
}
__device__ __forceinline__ bool tag_ok(v4u p, unsigned T) { return (p.y == T) & (p.w == ~T); }
__device__ __forceinline__ v4u mkpair(float a, float b, unsigned T) { return v4u{__float_as_uint(a), T, __float_as_uint(b), ~T}; }

#define SPIN_PAUSE(n) do { __builtin_amdgcn_s_sleep(n); asm volatile("" :: "s"(ws) : "memory"); } while (0)
#define STAMPW(i, cond) do { } while (0)

constexpr int L_XS = 0;
constexpr int L_Q = 4608;
constexpr int L_ACT = 13312 + 8192;
constexpr int L_ML = 6656;
constexpr int L_GML = 6784;
constexpr int L_FLAG = 7040;
constexpr int L_SCR = 7168;
constexpr int L_RED2 = 9216;
constexpr int L_RED = 13312;
constexpr int L_K = L_RED + 16384;
constexpr int L_BYTES = L_K + 131072;

__global__ void __launch_bounds__(768, 3)
fused_kernel(const u16* __restrict__ x, const u16* __restrict__ kc, const u16* __restrict__ vc, const u16* __restrict__ wq, const u16* __restrict__ wo,
             const u16* __restrict__ w1, const u16* __restrict__ b1, const u16* __restrict__ w2, const u16* __restrict__ b2,
             u16* __restrict__ out, unsigned char* ws) {
    __shared__ __attribute__((aligned(16))) unsigned char lds[L_BYTES];
    const int tid = threadIdx.x, lane = tid & 63, w = __builtin_amdgcn_readfirstlane(tid >> 6), bid = blockIdx.x;
    unsigned long long pc0; asm volatile("s_getpc_b64 %0" : "=s"(pc0));
    STAMPW(0, w == 0);
    const int s = bid & 7, b = (bid >> 3) & 1, kvh = bid >> 4;
    const int kq = (bid & 15) >> 2, cq = bid & 3;
    const bool has_w1 = bid < 216;
    const int c1 = bid % 72, k1 = bid / 72;
    const bool role_wo = bid < 144, role_w2 = bid >= 144 && bid < 252;
    const int u2 = bid - 144;
    const int tcg = role_wo ? bid % 9 : u2 % 9;
    const int trg = role_wo ? bid / 9 : u2 / 9;
    const int trows = role_wo ? 256 : 384;
    const __amdgpu_buffer_rsrc_t rws = RSRC(ws, WS_END);
    volatile LAS unsigned* qflag = (volatile LAS unsigned*)(LAS unsigned char*)(lds + L_FLAG);
    if (tid < 8) qflag[tid] = 0u;

    const int lrow = lane >> 3, lcol = (lane & 7) * 16;
    const unsigned hm = (lane & 1) ? 0xffff0000u : 0x0000ffffu;
    const int dsel = (lane >> 1) & 3;
    const unsigned M0 = dsel == 0 ? hm : 0u, M1 = dsel == 1 ? hm : 0u, M2 = dsel == 2 ? hm : 0u, M3 = dsel == 3 ? hm : 0u;
#define BDIAG(dup) v4u{(dup) & M0, (dup) & M1, (dup) & M2, (dup) & M3}
    unsigned T;

    v4u xr[3], Wf[24];
    if (w == 8 || w == 9) {
        __builtin_amdgcn_s_setprio(3);
        const __amdgpu_buffer_rsrc_t rx = RSRC(x, 2 * D * 2);
        const __amdgpu_buffer_rsrc_t rw = w == 8 ? RSRC(wq + (size_t)(144 * kq) * 4096 + 256 * kvh + 64 * cq, 144 * 4096 * 2 - 256 * kvh * 2 - 128 * cq)
                                                 : RSRC(w1 + (size_t)(192 * k1) * F + 64 * c1, has_w1 ? 192 * F * 2 - 128 * c1 : 0);
        const int ld = w == 8 ? 4096 * 2 : F * 2;
        const int nld = w == 8 ? 18 : 24;
#pragma unroll
        for (int j = 0; j < 3; ++j) xr[j] = __builtin_amdgcn_raw_buffer_load_b128(rx, (lane + 64 * j) * 16, 0, 0);
#pragma unroll
        for (int j = 0; j < 24; ++j) Wf[j] = LDNT(rw, j < nld ? (8 * j + lrow) * ld + lcol : OOB);
    }
    if (w >= 10) {
        const __amdgpu_buffer_rsrc_t rc = RSRC(pc0 & ~255ull, CODEPF * 1024);
#pragma unroll
        for (int i = 0; i < CODEPF / 2; ++i) Wf[i] = __builtin_amdgcn_raw_buffer_load_b128(rc, ((w - 10) * (CODEPF / 2) + i) * 1024 + lane * 16, 0, 0);
    }
    __builtin_amdgcn_sched_barrier(0);
    __syncthreads();
    __builtin_amdgcn_sched_barrier(0);

    if (__builtin_expect(w < 8, 1)) {
        const __amdgpu_buffer_rsrc_t rk = RSRC(kc + ((size_t)(b * HKV + kvh) * SK + (SK - WIN) + s * 512 + w * 64) * DH, 64 * 256);
        const __amdgpu_buffer_rsrc_t rv = RSRC(vc + ((size_t)(b * HKV + kvh) * SK + (SK - WIN) + s * 512 + w * 64) * DH, 64 * 256);
        v4u K[16], V[16];
        __builtin_amdgcn_s_setprio(1);
#pragma unroll
        for (int j = 0; j < 16; ++j) K[j] = LDNT(rk, j * 1024 + lane * 16);
        __builtin_amdgcn_sched_barrier(0);
        __builtin_amdgcn_s_setprio(0);
        __builtin_amdgcn_sched_barrier(0);
#pragma unroll
        for (int j = 0; j < 16; ++j) V[j] = LDNT(rv, j * 1024 + lane * 16);
        __builtin_amdgcn_sched_barrier(0);
        if (lane == 0) __hip_atomic_fetch_add((LAS unsigned*)(qflag + 5), 1u, __ATOMIC_RELAXED, __HIP_MEMORY_SCOPE_WORKGROUP);
        STAMPW(1, w == 0); STAMPW(2, w == 7);
        const int g = lane >> 4, c = lane & 15;
#pragma unroll
        for (int j = 0; j < 16; ++j) { const int i = 4 * (j & 3) + g; *(v4u*)(lds + L_K + w * 16384 + (j >> 2) * 4096 + i * 256 + ((c ^ (4 * g + (j & 3))) * 16)) = K[j]; }
        { unsigned spin = 0; while (*qflag == 0u && ++spin < (SPIN_CAP << 4)) __builtin_amdgcn_s_sleep(1); T = qflag[1]; }
        asm volatile("s_waitcnt lgkmcnt(0)" ::: "memory");
        STAMPW(3, w == 0); STAMPW(4, w == 7);
        v4u qf[4];
#pragma unroll
        for (int ks = 0; ks < 4; ++ks) qf[ks] = *(const v4u*)(lds + L_Q + (c >> 3) * 256 + (4 * ks + g) * 16);
        f32x4 sc[4];
#pragma unroll
        for (int t = 0; t < 4; ++t) {
            sc[t] = f32x4{0, 0, 0, 0};
#pragma unroll
            for (int ks = 0; ks < 4; ++ks) {
                const v4u ka = *(const v4u*)(lds + L_K + w * 16384 + t * 4096 + (4 * (c & 3) + (c >> 2)) * 256 + (((4 * ks + g) ^ c) * 16));
                sc[t] = mfma(ka, qf[ks], sc[t]);
            }
        }
        float m = -INFINITY;
#pragma unroll
        for (int t = 0; t < 4; ++t)
#pragma unroll
            for (int r = 0; r < 4; ++r) { sc[t][r] = rbf(rbf(sc[t][r]) * 0.088388347648318440f); m = fmaxf(m, sc[t][r]); }
        m = xrow_max(m);
        float l = 0.f;
        constexpr float LOG2E = 1.4426950408889634f;
        const float mm = m * LOG2E;
        unsigned pd[16];
#pragma unroll
        for (int t = 0; t < 4; ++t)
#pragma unroll
            for (int r = 0; r < 4; ++r) { const float p = __builtin_amdgcn_exp2f(sc[t][r] * LOG2E - mm); l += p; pd[4 * t + r] = cvtpk(p, p); }
        l = xrow_sum(l);
        STAMPW(5, w == 7);
        f32x4 acc0 = {0, 0, 0, 0}, acc1 = {0, 0, 0, 0};
#pragma unroll
        for (int t = 0; t < 16; t += 2) { acc0 = mfma(V[t], BDIAG(pd[t]), acc0); acc1 = mfma(V[t + 1], BDIAG(pd[t + 1]), acc1); }
        acc0 += acc1;
        __builtin_amdgcn_s_setprio(3);
        const int hh = (lane >> 3) & 1;
        *(f32x4*)(lds + L_RED + (w * 64 + lane) * 16) = acc0;
        if ((lane & 0x37) == 0) *(float2*)(lds + L_ML + (w * 2 + hh) * 8) = make_float2(mm, l);
        asm volatile("s_waitcnt lgkmcnt(0)" ::: "memory");
        unsigned arr = 0;
        if (lane == 0) arr = __hip_atomic_fetch_add((LAS unsigned*)(qflag + 2), 1u, __ATOMIC_RELAXED, __HIP_MEMORY_SCOPE_WORKGROUP);
        arr = __builtin_amdgcn_readfirstlane(arr);
        asm volatile("" ::: "memory");
        if (arr == 7u) {
            float2 ml[8]; f32x4 pa[8];
#pragma unroll
            for (int i = 0; i < 8; ++i) { ml[i] = *(const float2*)(lds + L_ML + (i * 2 + hh) * 8); pa[i] = *(const f32x4*)(lds + L_RED + (i * 64 + lane) * 16); }
            float M = ml[0].x;
#pragma unroll
            for (int i = 1; i < 8; ++i) M = fmaxf(M, ml[i].x);
            f32x4 a = {0, 0, 0, 0}; float L = 0.f;
#pragma unroll
            for (int i = 0; i < 8; ++i) { const float wt = __builtin_amdgcn_exp2f(ml[i].x - M); a += wt * pa[i]; L += wt * ml[i].y; }
            const int off = WS_AP + bid * AP_REC + lane * 16;
            STSC(mkpair(a[0], a[1], T), rws, off);
            STSC(mkpair(a[2], a[3], T), rws, off + 1024);
            STSC(mkpair(M, L, T), rws, (lane & 0x37) == 0 ? WS_AP + bid * AP_REC + (128 + hh) * 16 : OOB);
            if (lane == 0) qflag[6] = 1u;
        }
        if (role_wo && w < 4) {
            { unsigned spin = 0; while (qflag[6] == 0u && ++spin < (SPIN_CAP << 4)) __builtin_amdgcn_s_sleep(2); }
            const int bb = (w >> 1) & 1, h2 = w & 1;
            const unsigned bit = 1u << (w & 3);
            const int base = WS_AP + (trg * 16 + bb * 8) * AP_REC;
            v4u g[8], gm[8];
            bool ok = false;
            unsigned spin = 0;
            do {
                if (qflag[3] & bit) break;
#pragma unroll
                for (int i = 0; i < 8; ++i) { g[i] = LDSC(rws, base + i * AP_REC + ((lane >> 5) * 64 + ((lane >> 3) & 3) * 16 + h2 * 8 + (lane & 7)) * 16); gm[i] = LDSC(rws, base + i * AP_REC + (128 + h2) * 16); }
                bool o = true;
#pragma unroll
                for (int i = 0; i < 8; ++i) o &= tag_ok(g[i], T) & tag_ok(gm[i], T);
                ok = __all(o);
                if (ok) break;
                SPIN_PAUSE(HSLEEP);
            } while (++spin < SPIN_CAP);
            if (ok) {
                float M = __uint_as_float(gm[0].x);
#pragma unroll
                for (int i = 1; i < 8; ++i) M = fmaxf(M, __uint_as_float(gm[i].x));
                float a0 = 0.f, a1 = 0.f, L = 0.f;
#pragma unroll
                for (int i = 0; i < 8; ++i) {
                    const float wt = __builtin_amdgcn_exp2f(__uint_as_float(gm[i].x) - M);
                    a0 += wt * __uint_as_float(g[i].x); a1 += wt * __uint_as_float(g[i].z); L += wt * __uint_as_float(gm[i].z);
                }
                const float ri = 1.f / L;
                const int dl = h2 * 128 + 32 * ((lane >> 3) & 3) + 16 * (lane >> 5) + (lane & 7);
                unsigned* act = (unsigned*)(lds + L_ACT);
                act[dl * 2 + bb] = cvtpk(a0 * ri, a0 * ri); act[(dl + 8) * 2 + bb] = cvtpk(a1 * ri, a1 * ri);
                asm volatile("s_waitcnt lgkmcnt(0)" ::: "memory");
                if (lane == 0) __hip_atomic_fetch_or((LAS unsigned*)(qflag + 3), bit, __ATOMIC_RELAXED, __HIP_MEMORY_SCOPE_WORKGROUP);
            } else if (spin >= SPIN_CAP) {
                if (lane == 0) __hip_atomic_fetch_or((LAS unsigned*)(qflag + 3), bit, __ATOMIC_RELAXED, __HIP_MEMORY_SCOPE_WORKGROUP);
            }
        }
    } else {
        const int sw = w - 8;
        if (w >= 10) {
#pragma unroll
            for (int i = 0; i < CODEPF / 2; ++i) asm volatile("" :: "v"(Wf[i]));
        }
        const __amdgpu_buffer_rsrc_t rt = RSRC(role_wo ? wo : w2, role_wo ? 4096 * D * 2 : (role_w2 ? F * D * 2 : 0));
        if (sw < 2) {
            const int row0 = sw == 0 ? 144 * kq : 192 * k1;
            unsigned char* xsb = lds + L_XS + sw * 2304;
            {
                float xv[3][8];
#pragma unroll
                for (int j = 0; j < 3; ++j)
#pragma unroll
                    for (int k = 0; k < 4; ++k) { xv[j][2 * k] = bf2f(xr[j][k] & 0xffffu); xv[j][2 * k + 1] = __uint_as_float(xr[j][k] & 0xffff0000u); }
                float sj[3];
#pragma unroll
                for (int j = 0; j < 3; ++j) { float t = 0.f;
#pragma unroll
                    for (int k = 0; k < 8; ++k) t += xv[j][k];
                    sj[j] = t; }
                const bool c1b0 = lane < 8, c2ok = lane < 16;
                const float mean0 = wave_sum(sj[0] + (c1b0 ? sj[1] : 0.f)) * (1.f / D);
                const float mean1 = wave_sum((c1b0 ? 0.f : sj[1]) + sj[2]) * (1.f / D);
                const float mj[3] = {mean0, c1b0 ? mean0 : mean1, mean1};
#pragma unroll
                for (int j = 0; j < 3; ++j) { float t = 0.f;
#pragma unroll
                    for (int k = 0; k < 8; ++k) { const float d = xv[j][k] - mj[j]; t += d * d; }
                    sj[j] = t; }
                if (!c2ok) sj[2] = 0.f;
                const float var0 = wave_sum(sj[0] + (c1b0 ? sj[1] : 0.f)) * (1.f / D);
                const float var1 = wave_sum((c1b0 ? 0.f : sj[1]) + sj[2]) * (1.f / D);
                const float mb0 = rbf(mean0), mb1 = rbf(mean1);
                const float den0 = rbf(sqrtf(rbf(rbf(var0) + 1e-5f))), den1 = rbf(sqrtf(rbf(rbf(var1) + 1e-5f)));
                const float ri0 = 1.f / den0, ri1 = 1.f / den1;
                const float mbj[3] = {mb0, c1b0 ? mb0 : mb1, mb1}, dnj[3] = {ri0, c1b0 ? ri0 : ri1, ri1};
#pragma unroll
                for (int j = 0; j < 3; ++j) {
                    v4u o;
#pragma unroll
                    for (int k = 0; k < 4; ++k) o[k] = cvtpk(rbf(xv[j][2 * k] - mbj[j]) * dnj[j], rbf(xv[j][2 * k + 1] - mbj[j]) * dnj[j]);
                    if (j < 2 || c2ok) *(v4u*)(xsb + (lane + 64 * j) * 16) = o;
                }
            }
            __builtin_amdgcn_fence(__ATOMIC_ACQ_REL, "wavefront");
            asm volatile("" ::: "memory");
            STAMPW(10, sw == 0);
            T = *(const __attribute__((address_space(4))) unsigned*)(uintptr_t)(ws + WS_E) + 1u; asm volatile("" : "+s"(T));
            const u16* xs = (const u16*)xsb;
            f32x4 af[2] = {{0, 0, 0, 0}, {0, 0, 0, 0}};
#pragma unroll
            for (int j = 0; j < 24; ++j) {
                const int r = min(row0 + 8 * j + lrow, D - 1);
#pragma unroll
                for (int bb = 0; bb < 2; ++bb) { const unsigned v = xs[bb * D + r]; const unsigned dup = v | (v << 16); af[bb] = mfma(Wf[j], BDIAG(dup), af[bb]); }
            }
            float* scr = (float*)(lds + L_SCR + sw * 1024);
            if ((lane >> 5) == ((lane >> 3) & 1)) {
                const int r1 = lane >> 5, cb = 32 * ((lane >> 4) & 1) + (lane & 7);
#pragma unroll
                for (int bb = 0; bb < 2; ++bb)
#pragma unroll
                    for (int r = 0; r < 4; ++r) scr[(r1 * 2 + bb) * 64 + cb + 8 * r] = af[bb][r];
            }
            __builtin_amdgcn_fence(__ATOMIC_ACQ_REL, "wavefront");
            asm volatile("" ::: "memory");
            {
                const int bb = lane >> 5, pr = lane & 31;
                const float2 t0 = *(const float2*)&scr[bb * 64 + 2 * pr], t1 = *(const float2*)&scr[(2 + bb) * 64 + 2 * pr];
                const int off = sw == 0 ? WS_QP + (((kvh * 2 + bb) * 4 + kq) * 128 + cq * 32 + pr) * 16
                                        : (has_w1 ? WS_HP + (((c1 * 2 + bb) * 3 + k1) * 32 + pr) * 16 : OOB);
                STSC(mkpair(t0.x + t1.x, t0.y + t1.y, T), rws, off);
            }
            STAMPW(11, sw == 0);
            if (sw == 0) {
                const int qoff = WS_QP + ((kvh * 2 + b) * 4 * 128 + lane) * 16;
                v4u gq[8]; bool ok; unsigned spin = 0;
                do {
#pragma unroll
                    for (int i = 0; i < 8; ++i) gq[i] = LDSC(rws, qoff + (i >> 1) * 128 * 16 + (i & 1) * 64 * 16);
                    ok = true;
#pragma unroll
                    for (int i = 0; i < 8; ++i) ok &= tag_ok(gq[i], T);
                    if (__all(ok)) break;
                    SPIN_PAUSE(1);
                } while (++spin < SPIN_CAP);
#pragma unroll
                for (int hf2 = 0; hf2 < 2; ++hf2) {
                    const float q0 = (__uint_as_float(gq[hf2].x) + __uint_as_float(gq[2 + hf2].x)) + (__uint_as_float(gq[4 + hf2].x) + __uint_as_float(gq[6 + hf2].x));
                    const float q1 = (__uint_as_float(gq[hf2].z) + __uint_as_float(gq[2 + hf2].z)) + (__uint_as_float(gq[4 + hf2].z) + __uint_as_float(gq[6 + hf2].z));
                    ((unsigned*)(lds + L_Q))[hf2 * 64 + lane] = cvtpk(q0, q1);
                }
                if (lane == 0) qflag[1] = T;
                asm volatile("s_waitcnt lgkmcnt(0)" ::: "memory");
                if (lane == 0) *qflag = 1u;
                STAMPW(12, true);
            }
            __builtin_amdgcn_s_setprio(0);
        }
        T = *(const __attribute__((address_space(4))) unsigned*)(uintptr_t)(ws + WS_E) + 1u; asm volatile("" : "+s"(T));
        __builtin_amdgcn_sched_barrier(0);
        { unsigned spin = 0; while (qflag[5] < 8u && ++spin < (SPIN_CAP << 4)) __builtin_amdgcn_s_sleep(2); }
        v4u Wt[12];
#pragma unroll
        for (int j = 0; j < 12; ++j) { const int u = sw + 4 * j; Wt[j] = LDNT(rt, (8 * u < trows) ? ((trows * trg + 8 * u + lrow) * D + 64 * tcg) * 2 + lcol : OOB); }
        __builtin_amdgcn_sched_barrier(0);
        STAMPW(13, sw == 3);
        if (role_w2) {
            const int st = tid - 512;
            const __amdgpu_buffer_rsrc_t rb1 = RSRC(b1, F * 2);
            v4u gh[2][3]; unsigned b1v[2]; int hoff[2];
#pragma unroll
            for (int e = 0; e < 2; ++e) {
                const int it = st + 256 * e; const bool valid = it < 384;
                const int hbb = it / 192, hpr = it % 192, hf = 384 * trg + 2 * hpr;
                hoff[e] = valid ? WS_HP + ((((hf >> 6) * 2 + hbb) * 3) * 32 + ((hf & 63) >> 1)) * 16 : OOB;
                b1v[e] = __builtin_amdgcn_raw_buffer_load_b32(rb1, valid ? hf * 2 : OOB, 0, 0);
            }
            bool ok; unsigned spin = 0;
            do {
                ok = true;
#pragma unroll
                for (int e = 0; e < 2; ++e)
#pragma unroll
                    for (int i = 0; i < 3; ++i) { gh[e][i] = LDSC(rws, hoff[e] == OOB ? OOB : hoff[e] + i * 32 * 16); }
#pragma unroll
                for (int e = 0; e < 2; ++e)
#pragma unroll
                    for (int i = 0; i < 3; ++i) ok &= (hoff[e] == OOB) | tag_ok(gh[e][i], T);
                if (__all(ok)) break;
                SPIN_PAUSE(2);
            } while (++spin < SPIN_CAP);
#pragma unroll
            for (int e = 0; e < 2; ++e) {
                const int it = st + 256 * e;
                float h0 = __uint_as_float(gh[e][0].x) + __uint_as_float(gh[e][1].x) + __uint_as_float(gh[e][2].x);
                float h1 = __uint_as_float(gh[e][0].z) + __uint_as_float(gh[e][1].z) + __uint_as_float(gh[e][2].z);
                h0 = rbf(rbf(h0) + bf2f(b1v[e] & 0xffffu)); h1 = rbf(rbf(h1) + __uint_as_float(b1v[e] & 0xffff0000u));
                h0 = h0 > 0.f ? h0 : 0.f; h1 = h1 > 0.f ? h1 : 0.f;
                h0 *= h0; h1 *= h1;
                const int hbb = it / 192, hpr = it % 192;
                if (it < 384) { unsigned* act = (unsigned*)(lds + L_ACT); act[(2 * hpr) * 2 + hbb] = cvtpk(h0, h0); act[(2 * hpr + 1) * 2 + hbb] = cvtpk(h1, h1); }
            }
            asm volatile("s_waitcnt lgkmcnt(0)" ::: "memory");
            if (lane == 0) __hip_atomic_fetch_or((LAS unsigned*)(qflag + 3), 1u << sw, __ATOMIC_RELAXED, __HIP_MEMORY_SCOPE_WORKGROUP);
        }
        if (role_wo || role_w2) {
            { unsigned spin = 0; while (qflag[3] != 15u && ++spin < (SPIN_CAP << 4)) __builtin_amdgcn_s_sleep(0); }
            asm volatile("" ::: "memory");
            const unsigned* act = (const unsigned*)(lds + L_ACT);
            f32x4 at[2][2] = {{{0, 0, 0, 0}, {0, 0, 0, 0}}, {{0, 0, 0, 0}, {0, 0, 0, 0}}};
            typedef unsigned v2u __attribute__((ext_vector_type(2)));
            v2u ad[8];
#pragma unroll
            for (int j = 0; j < 8; ++j) ad[j] = *(const v2u*)&act[(8 * (sw + 4 * j) + lrow) * 2];
#pragma unroll
            for (int j = 0; j < 8; ++j) { at[0][j & 1] = mfma(Wt[j], BDIAG(ad[j].x), at[0][j & 1]); at[1][j & 1] = mfma(Wt[j], BDIAG(ad[j].y), at[1][j & 1]); }
            if (role_w2) {
#pragma unroll
                for (int j = 8; j < 12; ++j) { const v2u a2 = *(const v2u*)&act[(8 * (sw + 4 * j) + lrow) * 2]; at[0][j & 1] = mfma(Wt[j], BDIAG(a2.x), at[0][j & 1]); at[1][j & 1] = mfma(Wt[j], BDIAG(a2.y), at[1][j & 1]); }
            }
            at[0][0] += at[0][1]; at[1][0] += at[1][1];
            float* red2 = (float*)(lds + L_RED2);
            if ((lane >> 5) == ((lane >> 3) & 1)) {
                const int r1 = lane >> 5, cb = 32 * ((lane >> 4) & 1) + (lane & 7);
#pragma unroll
                for (int bb = 0; bb < 2; ++bb)
#pragma unroll
                    for (int r = 0; r < 4; ++r) red2[((sw * 2 + r1) * 2 + bb) * 64 + cb + 8 * r] = at[bb][0][r];
            }
            asm volatile("s_waitcnt lgkmcnt(0)" ::: "memory");
            unsigned arr = 0;
            if (lane == 0) arr = __hip_atomic_fetch_add((LAS unsigned*)(qflag + 4), 1u, __ATOMIC_RELAXED, __HIP_MEMORY_SCOPE_WORKGROUP);
            arr = __builtin_amdgcn_readfirstlane(arr);
            asm volatile("" ::: "memory");
            if (arr == 3u) {
                const int bb = lane >> 5, pr = lane & 31;
                float v0 = 0.f, v1 = 0.f;
#pragma unroll
                for (int i = 0; i < 8; ++i) { const float2 t = *(const float2*)&red2[(i * 2 + bb) * 64 + 2 * pr]; v0 += t.x; v1 += t.y; }
                const int off = role_wo ? WS_SP + (((tcg * 2 + bb) * 16 + trg) * 32 + pr) * 16 : WS_MP + (((tcg * 2 + bb) * 12 + trg) * 32 + pr) * 16;
                STSC(mkpair(v0, v1, T), rws, off);
            }
        }
    }
    STAMPW(9, w == 0);

    if (bid >= 144 && bid < 216 && w >= 1 && w <= NFIN) {
        if (w == 2) __builtin_amdgcn_s_sleep(STAG); if (w == 3) __builtin_amdgcn_s_sleep(STAG / 2);
        const int u = bid - 144, cg = u >> 3, fb = (u >> 2) & 1, q4 = u & 3;
        const int p8 = lane >> 3, j = lane & 7;
        const int col = 64 * cg + 16 * q4 + 2 * j;
        const unsigned xw = *(const unsigned*)(x + fb * D + col), bw = *(const unsigned*)(b2 + col);
        const int offs = WS_SP + (((cg * 2 + fb) * 16 + p8) * 32 + 8 * q4 + j) * 16;
        const int offm = WS_MP + (((cg * 2 + fb) * 12 + p8) * 32 + 8 * q4 + j) * 16;
        v4u g[4]; bool ok; unsigned spin = 0;
        do {
            g[0] = LDSC(rws, offs); g[1] = LDSC(rws, offs + 8 * 32 * 16); g[2] = LDSC(rws, offm); g[3] = LDSC(rws, p8 < 4 ? offm + 8 * 32 * 16 : OOB);
            ok = tag_ok(g[0], T) & tag_ok(g[1], T) & tag_ok(g[2], T) & ((p8 >= 4) | tag_ok(g[3], T));
            if (__all(ok)) break;
            SPIN_PAUSE(1);
        } while (++spin < SPIN_CAP);
        float s0 = __uint_as_float(g[0].x) + __uint_as_float(g[1].x), s1 = __uint_as_float(g[0].z) + __uint_as_float(g[1].z);
        float m0 = __uint_as_float(g[2].x) + __uint_as_float(g[3].x), m1 = __uint_as_float(g[2].z) + __uint_as_float(g[3].z);
        s0 += dpp<0x128>(s0); s1 += dpp<0x128>(s1); m0 += dpp<0x128>(m0); m1 += dpp<0x128>(m1);
        s0 = xrow_sum(s0); s1 = xrow_sum(s1); m0 = xrow_sum(m0); m1 = xrow_sum(m1);
        if (lane < 8) {
            const float mv0 = rbf(rbf(m0) + bf2f(bw & 0xffffu)), mv1 = rbf(rbf(m1) + __uint_as_float(bw & 0xffff0000u));
            const float g0 = rbf(mv0 / (1.f + __expf(-mv0))), g1 = rbf(mv1 / (1.f + __expf(-mv1)));
            *(unsigned*)(out + fb * D + col) = cvtpk(bf2f(xw & 0xffffu) + rbf(rbf(s0) * g0), __uint_as_float(xw & 0xffff0000u) + rbf(rbf(s1) * g1));
        }
        if (u == 0 && lane == 0 && w == 1) __builtin_amdgcn_raw_buffer_store_b32(T, rws, WS_E, 0, 16);
        STAMPW(14, true);
    }
    STAMPW(14, w == 0);
}

extern "C" void kernel_launch(void* const* d_in, const int* in_sizes, int n_in, void* d_out, int out_size, void* d_ws, size_t ws_size, hipStream_t stream) {
    fused_kernel<<<256, 768, 0, stream>>>((const u16*)d_in[0], (const u16*)d_in[1], (const u16*)d_in[2], (const u16*)d_in[3], (const u16*)d_in[4],
                                          (const u16*)d_in[6], (const u16*)d_in[7], (const u16*)d_in[8], (const u16*)d_in[9], (u16*)d_out, (unsigned char*)d_ws);
}
```

```cpp
#include <hip/hip_runtime.h>
#include <stdint.h>

typedef unsigned short u16;
typedef unsigned v4u __attribute__((ext_vector_type(4)));
typedef float f32x4 __attribute__((ext_vector_type(4)));
typedef short bf16x8 __attribute__((ext_vector_type(8)));
typedef __bf16 bf16x2 __attribute__((ext_vector_type(2)));
#define LAS __attribute__((address_space(3)))
#define STAG 14
#define NFIN 1
#define HSLEEP 2
#define CODEPF 16
#define B0MODE 0

constexpr int D = 576, HKV = 16, SK = 16384, DH = 128, WIN = 4096, F = 4608;
constexpr int WS_E = 0;
constexpr int WS_QP = 4096;
constexpr int WS_HP = WS_QP + 16 * 2 * 4 * 128 * 16;
constexpr int WS_AP = WS_HP + 72 * 2 * 3 * 32 * 16;
constexpr int AP_REC = 136 * 16;
constexpr int WS_SP = WS_AP + 256 * AP_REC;
constexpr int WS_MP = WS_SP + 9 * 2 * 16 * 32 * 16;
constexpr int WS_END = WS_MP + 9 * 2 * 12 * 32 * 16;
constexpr int WS_STAMP = (WS_END + 4095) / 4096 * 4096;
constexpr unsigned SPIN_CAP = 1u << 18;
constexpr int OOB = 0x7ffffff0;

#define RSRC(p, n) __builtin_amdgcn_make_buffer_rsrc((void*)(p), 0, (n), 0x00020000)
#define AUXK 2
#define AUXV 2
#define AUXW 2
#define LDNT(rs, off) __builtin_amdgcn_raw_buffer_load_b128(rs, off, 0, AUXW)
#define LDK(rs, off) __builtin_amdgcn_raw_buffer_load_b128(rs, off, 0, AUXK)
#define LDV(rs, off) __builtin_amdgcn_raw_buffer_load_b128(rs, off, 0, AUXV)
#define LDSC(rs, off) __builtin_amdgcn_raw_buffer_load_b128(rs, off, 0, 16)
#define STSC(v, rs, off) __builtin_amdgcn_raw_buffer_store_b128(v, rs, off, 0, 16)

__device__ __forceinline__ float bf2f(unsigned b) { return __uint_as_float(b << 16); }
typedef float f32x2 __attribute__((ext_vector_type(2)));
__device__ __forceinline__ unsigned cvtpk(float lo, float hi) { const f32x2 v = {lo, hi}; return __builtin_bit_cast(unsigned, __builtin_convertvector(v, bf16x2)); }
__device__ __forceinline__ float rbf(float f) { return __uint_as_float(cvtpk(f, f) << 16); }
__device__ __forceinline__ float dot2(unsigned k, unsigned q, float acc) {
    return __builtin_amdgcn_fdot2_f32_bf16(__builtin_bit_cast(bf16x2, k), __builtin_bit_cast(bf16x2, q), acc, false);
}
template <int CTRL> __device__ __forceinline__ float dpp(float x) {
    return __builtin_bit_cast(float, __builtin_amdgcn_mov_dpp(__builtin_bit_cast(int, x), CTRL, 0xf, 0xf, true));
}
__device__ __forceinline__ f32x4 mfma(v4u a, v4u b, f32x4 c) {
    return __builtin_amdgcn_mfma_f32_16x16x32_bf16(__builtin_bit_cast(bf16x8, a), __builtin_bit_cast(bf16x8, b), c, 0, 0, 0);
}
__device__ __forceinline__ float xrow_sum(float x) {
    auto s = __builtin_amdgcn_permlane16_swap(__float_as_uint(x), __float_as_uint(x), false, false);
    x = __uint_as_float(s[0]) + __uint_as_float(s[1]);
    auto t = __builtin_amdgcn_permlane32_swap(__float_as_uint(x), __float_as_uint(x), false, false);
    return __uint_as_float(t[0]) + __uint_as_float(t[1]);
}
__device__ __forceinline__ float xrow_max(float x) {
    auto s = __builtin_amdgcn_permlane16_swap(__float_as_uint(x), __float_as_uint(x), false, false);
    x = fmaxf(__uint_as_float(s[0]), __uint_as_float(s[1]));
    auto t = __builtin_amdgcn_permlane32_swap(__float_as_uint(x), __float_as_uint(x), false, false);
    return fmaxf(__uint_as_float(t[0]), __uint_as_float(t[1]));
}
__device__ __forceinline__ float wave_sum(float v) {
    v += dpp<0xB1>(v); v += dpp<0x4E>(v); v += dpp<0x141>(v); v += dpp<0x140>(v);
    return xrow_sum(v);
}
__device__ __forceinline__ bool tag_ok(v4u p, unsigned T) { return (p.y == T) & (p.w == ~T); }
__device__ __forceinline__ v4u mkpair(float a, float b, unsigned T) { return v4u{__float_as_uint(a), T, __float_as_uint(b), ~T}; }

#define SPIN_PAUSE(n) do { __builtin_amdgcn_s_sleep(n); asm volatile("" :: "s"(ws) : "memory"); } while (0)
#define STAMPW(i, cond) do { } while (0)

constexpr int L_XS = 0;
constexpr int L_Q = 4608;
constexpr int L_ACT = 13312 + 8192;
constexpr int L_ML = 6656;
constexpr int L_GML = 6784;
constexpr int L_FLAG = 7040;
constexpr int L_SCR = 7168;
constexpr int L_RED2 = 9216;
constexpr int L_RED = 13312;
constexpr int L_K = L_RED + 16384;
constexpr int L_BYTES = L_K + 131072;

__global__ void __launch_bounds__(768, 3)
fused_kernel(const u16* __restrict__ x, const u16* __restrict__ kc, const u16* __restrict__ vc, const u16* __restrict__ wq, const u16* __restrict__ wo,
             const u16* __restrict__ w1, const u16* __restrict__ b1, const u16* __restrict__ w2, const u16* __restrict__ b2,
             u16* __restrict__ out, unsigned char* ws) {
    __shared__ __attribute__((aligned(16))) unsigned char lds[L_BYTES];
    const int tid = threadIdx.x, lane = tid & 63, w = __builtin_amdgcn_readfirstlane(tid >> 6), bid = blockIdx.x;
    unsigned long long pc0; asm volatile("s_getpc_b64 %0" : "=s"(pc0));
    STAMPW(0, w == 0);
    const int s = bid & 7, b = (bid >> 3) & 1, kvh = bid >> 4;
    const int kq = (bid & 15) >> 2, cq = bid & 3;
    const bool has_w1 = bid < 216;
    const int c1 = bid % 72, k1 = bid / 72;
    const bool role_wo = bid < 144, role_w2 = bid >= 144 && bid < 252;
    const int u2 = bid - 144;
    const int tcg = role_wo ? bid % 9 : u2 % 9;
    const int trg = role_wo ? bid / 9 : u2 / 9;
    const int trows = role_wo ? 256 : 384;
    const __amdgpu_buffer_rsrc_t rws = RSRC(ws, WS_END);
    volatile LAS unsigned* qflag = (volatile LAS unsigned*)(LAS unsigned char*)(lds + L_FLAG);
    if (tid < 8) qflag[tid] = 0u;

    const int lrow = lane >> 3, lcol = (lane & 7) * 16;
    const unsigned hm = (lane & 1) ? 0xffff0000u : 0x0000ffffu;
    const int dsel = (lane >> 1) & 3;
    const unsigned M0 = dsel == 0 ? hm : 0u, M1 = dsel == 1 ? hm : 0u, M2 = dsel == 2 ? hm : 0u, M3 = dsel == 3 ? hm : 0u;
#define BDIAG(dup) v4u{(dup) & M0, (dup) & M1, (dup) & M2, (dup) & M3}
    unsigned T;

    v4u xr[3], Wf[24];
    if (w == 8 || w == 9) {
        __builtin_amdgcn_s_setprio(3);
        const __amdgpu_buffer_rsrc_t rx = RSRC(x, 2 * D * 2);
        const __amdgpu_buffer_rsrc_t rw = w == 8 ? RSRC(wq + (size_t)(144 * kq) * 4096 + 256 * kvh + 64 * cq, 144 * 4096 * 2 - 256 * kvh * 2 - 128 * cq)
                                                 : RSRC(w1 + (size_t)(192 * k1) * F + 64 * c1, has_w1 ? 192 * F * 2 - 128 * c1 : 0);
        const int ld = w == 8 ? 4096 * 2 : F * 2;
        const int nld = w == 8 ? 18 : 24;
#pragma unroll
        for (int j = 0; j < 3; ++j) xr[j] = __builtin_amdgcn_raw_buffer_load_b128(rx, (lane + 64 * j) * 16, 0, 0);
#pragma unroll
        for (int j = 0; j < 24; ++j) Wf[j] = LDNT(rw, j < nld ? (8 * j + lrow) * ld + lcol : OOB);
    }
    if (w >= 10) {
        const __amdgpu_buffer_rsrc_t rc = RSRC(pc0 & ~255ull, CODEPF * 1024);
#pragma unroll
        for (int i = 0; i < CODEPF / 2; ++i) Wf[i] = __builtin_amdgcn_raw_buffer_load_b128(rc, ((w - 10) * (CODEPF / 2) + i) * 1024 + lane * 16, 0, 0);
    }
    __builtin_amdgcn_sched_barrier(0);
    __syncthreads();
    __builtin_amdgcn_sched_barrier(0);

    if (__builtin_expect(w < 8, 1)) {
        const __amdgpu_buffer_rsrc_t rk = RSRC(kc + ((size_t)(b * HKV + kvh) * SK + (SK - WIN) + s * 512 + w * 64) * DH, 64 * 256);
        const __amdgpu_buffer_rsrc_t rv = RSRC(vc + ((size_t)(b * HKV + kvh) * SK + (SK - WIN) + s * 512 + w * 64) * DH, 64 * 256);
        v4u K[16], V[16];
        __builtin_amdgcn_s_setprio(1);
#pragma unroll
        for (int j = 0; j < 16; ++j) K[j] = LDK(rk, j * 1024 + lane * 16);
        __builtin_amdgcn_sched_barrier(0);
        __builtin_amdgcn_s_setprio(0);
        __builtin_amdgcn_sched_barrier(0);
#pragma unroll
        for (int j = 0; j < 16; ++j) V[j] = LDV(rv, j * 1024 + lane * 16);
        __builtin_amdgcn_sched_barrier(0);
        if (lane == 0) __hip_atomic_fetch_add((LAS unsigned*)(qflag + 5), 1u, __ATOMIC_RELAXED, __HIP_MEMORY_SCOPE_WORKGROUP);
        STAMPW(1, w == 0); STAMPW(2, w == 7);
        const int g = lane >> 4, c = lane & 15;
#pragma unroll
        for (int j = 0; j < 16; ++j) { const int i = 4 * (j & 3) + g; *(v4u*)(lds + L_K + w * 16384 + (j >> 2) * 4096 + i * 256 + ((c ^ (4 * g + (j & 3))) * 16)) = K[j]; }
        { unsigned spin = 0; while (*qflag == 0u && ++spin < (SPIN_CAP << 4)) __builtin_amdgcn_s_sleep(1); T = qflag[1]; }
        asm volatile("s_waitcnt lgkmcnt(0)" ::: "memory");
        STAMPW(3, w == 0); STAMPW(4, w == 7);
        v4u qf[4];
#pragma unroll
        for (int ks = 0; ks < 4; ++ks) qf[ks] = *(const v4u*)(lds + L_Q + (c >> 3) * 256 + (4 * ks + g) * 16);
        f32x4 sc[4];
#pragma unroll
        for (int t = 0; t < 4; ++t) {
            sc[t] = f32x4{0, 0, 0, 0};
#pragma unroll
            for (int ks = 0; ks < 4; ++ks) {
                const v4u ka = *(const v4u*)(lds + L_K + w * 16384 + t * 4096 + (4 * (c & 3) + (c >> 2)) * 256 + (((4 * ks + g) ^ c) * 16));
                sc[t] = mfma(ka, qf[ks], sc[t]);
            }
        }
        float m = -INFINITY;
#pragma unroll
        for (int t = 0; t < 4; ++t)
#pragma unroll
            for (int r = 0; r < 4; ++r) { sc[t][r] = rbf(rbf(sc[t][r]) * 0.088388347648318440f); m = fmaxf(m, sc[t][r]); }
        m = xrow_max(m);
        float l = 0.f;
        constexpr float LOG2E = 1.4426950408889634f;
        const float mm = m * LOG2E;
        unsigned pd[16];
#pragma unroll
        for (int t = 0; t < 4; ++t)
#pragma unroll
            for (int r = 0; r < 4; ++r) { const float p = __builtin_amdgcn_exp2f(sc[t][r] * LOG2E - mm); l += p; pd[4 * t + r] = cvtpk(p, p); }
        l = xrow_sum(l);
        STAMPW(5, w == 7);
        f32x4 acc0 = {0, 0, 0, 0}, acc1 = {0, 0, 0, 0};
#pragma unroll
        for (int t = 0; t < 16; t += 2) { acc0 = mfma(V[t], BDIAG(pd[t]), acc0); acc1 = mfma(V[t + 1], BDIAG(pd[t + 1]), acc1); }
        acc0 += acc1;
        __builtin_amdgcn_s_setprio(3);
        const int hh = (lane >> 3) & 1;
        *(f32x4*)(lds + L_RED + (w * 64 + lane) * 16) = acc0;
        if ((lane & 0x37) == 0) *(float2*)(lds + L_ML + (w * 2 + hh) * 8) = make_float2(mm, l);
        asm volatile("s_waitcnt lgkmcnt(0)" ::: "memory");
        unsigned arr = 0;
        if (lane == 0) arr = __hip_atomic_fetch_add((LAS unsigned*)(qflag + 2), 1u, __ATOMIC_RELAXED, __HIP_MEMORY_SCOPE_WORKGROUP);
        arr = __builtin_amdgcn_readfirstlane(arr);
        asm volatile("" ::: "memory");
        if (arr == 7u) {
            float2 ml[8]; f32x4 pa[8];
#pragma unroll
            for (int i = 0; i < 8; ++i) { ml[i] = *(const float2*)(lds + L_ML + (i * 2 + hh) * 8); pa[i] = *(const f32x4*)(lds + L_RED + (i * 64 + lane) * 16); }
            float M = ml[0].x;
#pragma unroll
            for (int i = 1; i < 8; ++i) M = fmaxf(M, ml[i].x);
            f32x4 a = {0, 0, 0, 0}; float L = 0.f;
#pragma unroll
            for (int i = 0; i < 8; ++i) { const float wt = __builtin_amdgcn_exp2f(ml[i].x - M); a += wt * pa[i]; L += wt * ml[i].y; }
            const int off = WS_AP + bid * AP_REC + lane * 16;
            STSC(mkpair(a[0], a[1], T), rws, off);
            STSC(mkpair(a[2], a[3], T), rws, off + 1024);
            STSC(mkpair(M, L, T), rws, (lane & 0x37) == 0 ? WS_AP + bid * AP_REC + (128 + hh) * 16 : OOB);
            if (lane == 0) qflag[6] = 1u;
        }
        if (role_wo && w < 4) {
            { unsigned spin = 0; while (qflag[6] == 0u && ++spin < (SPIN_CAP << 4)) __builtin_amdgcn_s_sleep(2); }
            const int bb = (w >> 1) & 1, h2 = w & 1;
            const unsigned bit = 1u << (w & 3);
            const int base = WS_AP + (trg * 16 + bb * 8) * AP_REC;
            v4u g[8], gm[8];
            bool ok = false;
            unsigned spin = 0;
            do {
                if (qflag[3] & bit) break;
#pragma unroll
                for (int i = 0; i < 8; ++i) { g[i] = LDSC(rws, base + i * AP_REC + ((lane >> 5) * 64 + ((lane >> 3) & 3) * 16 + h2 * 8 + (lane & 7)) * 16); gm[i] = LDSC(rws, base + i * AP_REC + (128 + h2) * 16); }
                bool o = true;
#pragma unroll
                for (int i = 0; i < 8; ++i) o &= tag_ok(g[i], T) & tag_ok(gm[i], T);
                ok = __all(o);
                if (ok) break;
                SPIN_PAUSE(HSLEEP);
            } while (++spin < SPIN_CAP);
            if (ok) {
                float M = __uint_as_float(gm[0].x);
#pragma unroll
                for (int i = 1; i < 8; ++i) M = fmaxf(M, __uint_as_float(gm[i].x));
                float a0 = 0.f, a1 = 0.f, L = 0.f;
#pragma unroll
                for (int i = 0; i < 8; ++i) {
                    const float wt = __builtin_amdgcn_exp2f(__uint_as_float(gm[i].x) - M);
                    a0 += wt * __uint_as_float(g[i].x); a1 += wt * __uint_as_float(g[i].z); L += wt * __uint_as_float(gm[i].z);
                }
                const float ri = 1.f / L;
                const int dl = h2 * 128 + 32 * ((lane >> 3) & 3) + 16 * (lane >> 5) + (lane & 7);
                unsigned* act = (unsigned*)(lds + L_ACT);
                act[dl * 2 + bb] = cvtpk(a0 * ri, a0 * ri); act[(dl + 8) * 2 + bb] = cvtpk(a1 * ri, a1 * ri);
                asm volatile("s_waitcnt lgkmcnt(0)" ::: "memory");
                if (lane == 0) __hip_atomic_fetch_or((LAS unsigned*)(qflag + 3), bit, __ATOMIC_RELAXED, __HIP_MEMORY_SCOPE_WORKGROUP);
            } else if (spin >= SPIN_CAP) {
                if (lane == 0) __hip_atomic_fetch_or((LAS unsigned*)(qflag + 3), bit, __ATOMIC_RELAXED, __HIP_MEMORY_SCOPE_WORKGROUP);
            }
        }
    } else {
        const int sw = w - 8;
        if (w >= 10) {
#pragma unroll
            for (int i = 0; i < CODEPF / 2; ++i) asm volatile("" :: "v"(Wf[i]));
        }
        const __amdgpu_buffer_rsrc_t rt = RSRC(role_wo ? wo : w2, role_wo ? 4096 * D * 2 : (role_w2 ? F * D * 2 : 0));
        if (sw < 2) {
            const int row0 = sw == 0 ? 144 * kq : 192 * k1;
            unsigned char* xsb = lds + L_XS + sw * 2304;
            {
                float xv[3][8];
#pragma unroll
                for (int j = 0; j < 3; ++j)
#pragma unroll
                    for (int k = 0; k < 4; ++k) { xv[j][2 * k] = bf2f(xr[j][k] & 0xffffu); xv[j][2 * k + 1] = __uint_as_float(xr[j][k] & 0xffff0000u); }
                float sj[3];
#pragma unroll
                for (int j = 0; j < 3; ++j) { float t = 0.f;
#pragma unroll
                    for (int k = 0; k < 8; ++k) t += xv[j][k];
                    sj[j] = t; }
                const bool c1b0 = lane < 8, c2ok = lane < 16;
                const float mean0 = wave_sum(sj[0] + (c1b0 ? sj[1] : 0.f)) * (1.f / D);
                const float mean1 = wave_sum((c1b0 ? 0.f : sj[1]) + sj[2]) * (1.f / D);
                const float mj[3] = {mean0, c1b0 ? mean0 : mean1, mean1};
#pragma unroll
                for (int j = 0; j < 3; ++j) { float t = 0.f;
#pragma unroll
                    for (int k = 0; k < 8; ++k) { const float d = xv[j][k] - mj[j]; t += d * d; }
                    sj[j] = t; }
                if (!c2ok) sj[2] = 0.f;
                const float var0 = wave_sum(sj[0] + (c1b0 ? sj[1] : 0.f)) * (1.f / D);
                const float var1 = wave_sum((c1b0 ? 0.f : sj[1]) + sj[2]) * (1.f / D);
                const float mb0 = rbf(mean0), mb1 = rbf(mean1);
                const float den0 = rbf(sqrtf(rbf(rbf(var0) + 1e-5f))), den1 = rbf(sqrtf(rbf(rbf(var1) + 1e-5f)));
                const float ri0 = 1.f / den0, ri1 = 1.f / den1;
                const float mbj[3] = {mb0, c1b0 ? mb0 : mb1, mb1}, dnj[3] = {ri0, c1b0 ? ri0 : ri1, ri1};
#pragma unroll
                for (int j = 0; j < 3; ++j) {
                    v4u o;
#pragma unroll
                    for (int k = 0; k < 4; ++k) o[k] = cvtpk(rbf(xv[j][2 * k] - mbj[j]) * dnj[j], rbf(xv[j][2 * k + 1] - mbj[j]) * dnj[j]);
                    if (j < 2 || c2ok) *(v4u*)(xsb + (lane + 64 * j) * 16) = o;
                }
            }
            __builtin_amdgcn_fence(__ATOMIC_ACQ_REL, "wavefront");
            asm volatile("" ::: "memory");
            STAMPW(10, sw == 0);
            T = *(const __attribute__((address_space(4))) unsigned*)(uintptr_t)(ws + WS_E) + 1u; asm volatile("" : "+s"(T));
            const u16* xs = (const u16*)xsb;
            f32x4 af[2] = {{0, 0, 0, 0}, {0, 0, 0, 0}};
#pragma unroll
            for (int j = 0; j < 24; ++j) {
                const int r = min(row0 + 8 * j + lrow, D - 1);
#pragma unroll
                for (int bb = 0; bb < 2; ++bb) { const unsigned v = xs[bb * D + r]; const unsigned dup = v | (v << 16); af[bb] = mfma(Wf[j], BDIAG(dup), af[bb]); }
            }
            float* scr = (float*)(lds + L_SCR + sw * 1024);
            if ((lane >> 5) == ((lane >> 3) & 1)) {
                const int r1 = lane >> 5, cb = 32 * ((lane >> 4) & 1) + (lane & 7);
#pragma unroll
                for (int bb = 0; bb < 2; ++bb)
#pragma unroll
                    for (int r = 0; r < 4; ++r) scr[(r1 * 2 + bb) * 64 + cb + 8 * r] = af[bb][r];
            }
            __builtin_amdgcn_fence(__ATOMIC_ACQ_REL, "wavefront");
            asm volatile("" ::: "memory");
            {
                const int bb = lane >> 5, pr = lane & 31;
                const float2 t0 = *(const float2*)&scr[bb * 64 + 2 * pr], t1 = *(const float2*)&scr[(2 + bb) * 64 + 2 * pr];
                const int off = sw == 0 ? WS_QP + (((kvh * 2 + bb) * 4 + kq) * 128 + cq * 32 + pr) * 16
                                        : (has_w1 ? WS_HP + (((c1 * 2 + bb) * 3 + k1) * 32 + pr) * 16 : OOB);
                STSC(mkpair(t0.x + t1.x, t0.y + t1.y, T), rws, off);
            }
            STAMPW(11, sw == 0);
            if (sw == 0) {
                const int qoff = WS_QP + ((kvh * 2 + b) * 4 * 128 + lane) * 16;
                v4u gq[8]; bool ok; unsigned spin = 0;
                do {
#pragma unroll
                    for (int i = 0; i < 8; ++i) gq[i] = LDSC(rws, qoff + (i >> 1) * 128 * 16 + (i & 1) * 64 * 16);
                    ok = true;
#pragma unroll
                    for (int i = 0; i < 8; ++i) ok &= tag_ok(gq[i], T);
                    if (__all(ok)) break;
                    SPIN_PAUSE(1);
                } while (++spin < SPIN_CAP);
#pragma unroll
                for (int hf2 = 0; hf2 < 2; ++hf2) {
                    const float q0 = (__uint_as_float(gq[hf2].x) + __uint_as_float(gq[2 + hf2].x)) + (__uint_as_float(gq[4 + hf2].x) + __uint_as_float(gq[6 + hf2].x));
                    const float q1 = (__uint_as_float(gq[hf2].z) + __uint_as_float(gq[2 + hf2].z)) + (__uint_as_float(gq[4 + hf2].z) + __uint_as_float(gq[6 + hf2].z));
                    ((unsigned*)(lds + L_Q))[hf2 * 64 + lane] = cvtpk(q0, q1);
                }
                if (lane == 0) qflag[1] = T;
                asm volatile("s_waitcnt lgkmcnt(0)" ::: "memory");
                if (lane == 0) *qflag = 1u;
                STAMPW(12, true);
            }
            __builtin_amdgcn_s_setprio(0);
        }
        T = *(const __attribute__((address_space(4))) unsigned*)(uintptr_t)(ws + WS_E) + 1u; asm volatile("" : "+s"(T));
        __builtin_amdgcn_sched_barrier(0);
        { unsigned spin = 0; while (qflag[5] < 8u && ++spin < (SPIN_CAP << 4)) __builtin_amdgcn_s_sleep(2); }
        v4u Wt[12];
#pragma unroll
        for (int j = 0; j < 12; ++j) { const int u = sw + 4 * j; Wt[j] = LDNT(rt, (8 * u < trows) ? ((trows * trg + 8 * u + lrow) * D + 64 * tcg) * 2 + lcol : OOB); }
        __builtin_amdgcn_sched_barrier(0);
        STAMPW(13, sw == 3);
        if (role_w2) {
            const int st = tid - 512;
            const __amdgpu_buffer_rsrc_t rb1 = RSRC(b1, F * 2);
            v4u gh[2][3]; unsigned b1v[2]; int hoff[2];
#pragma unroll
            for (int e = 0; e < 2; ++e) {
                const int it = st + 256 * e; const bool valid = it < 384;
                const int hbb = it / 192, hpr = it % 192, hf = 384 * trg + 2 * hpr;
                hoff[e] = valid ? WS_HP + ((((hf >> 6) * 2 + hbb) * 3) * 32 + ((hf & 63) >> 1)) * 16 : OOB;
                b1v[e] = __builtin_amdgcn_raw_buffer_load_b32(rb1, valid ? hf * 2 : OOB, 0, 0);
            }
            bool ok; unsigned spin = 0;
            do {
                ok = true;
#pragma unroll
                for (int e = 0; e < 2; ++e)
#pragma unroll
                    for (int i = 0; i < 3; ++i) { gh[e][i] = LDSC(rws, hoff[e] == OOB ? OOB : hoff[e] + i * 32 * 16); }
#pragma unroll
                for (int e = 0; e < 2; ++e)
#pragma unroll
                    for (int i = 0; i < 3; ++i) ok &= (hoff[e] == OOB) | tag_ok(gh[e][i], T);
                if (__all(ok)) break;
                SPIN_PAUSE(2);
            } while (++spin < SPIN_CAP);
#pragma unroll
            for (int e = 0; e < 2; ++e) {
                const int it = st + 256 * e;
                float h0 = __uint_as_float(gh[e][0].x) + __uint_as_float(gh[e][1].x) + __uint_as_float(gh[e][2].x);
                float h1 = __uint_as_float(gh[e][0].z) + __uint_as_float(gh[e][1].z) + __uint_as_float(gh[e][2].z);
                h0 = rbf(rbf(h0) + bf2f(b1v[e] & 0xffffu)); h1 = rbf(rbf(h1) + __uint_as_float(b1v[e] & 0xffff0000u));
                h0 = h0 > 0.f ? h0 : 0.f; h1 = h1 > 0.f ? h1 : 0.f;
                h0 *= h0; h1 *= h1;
                const int hbb = it / 192, hpr = it % 192;
                if (it < 384) { unsigned* act = (unsigned*)(lds + L_ACT); act[(2 * hpr) * 2 + hbb] = cvtpk(h0, h0); act[(2 * hpr + 1) * 2 + hbb] = cvtpk(h1, h1); }
            }
            asm volatile("s_waitcnt lgkmcnt(0)" ::: "memory");
            if (lane == 0) __hip_atomic_fetch_or((LAS unsigned*)(qflag + 3), 1u << sw, __ATOMIC_RELAXED, __HIP_MEMORY_SCOPE_WORKGROUP);
        }
        if (role_wo || role_w2) {
            { unsigned spin = 0; while (qflag[3] != 15u && ++spin < (SPIN_CAP << 4)) __builtin_amdgcn_s_sleep(0); }
            asm volatile("" ::: "memory");
            const unsigned* act = (const unsigned*)(lds + L_ACT);
            f32x4 at[2][2] = {{{0, 0, 0, 0}, {0, 0, 0, 0}}, {{0, 0, 0, 0}, {0, 0, 0, 0}}};
            typedef unsigned v2u __attribute__((ext_vector_type(2)));
            v2u ad[8];
#pragma unroll
            for (int j = 0; j < 8; ++j) ad[j] = *(const v2u*)&act[(8 * (sw + 4 * j) + lrow) * 2];
#pragma unroll
            for (int j = 0; j < 8; ++j) { at[0][j & 1] = mfma(Wt[j], BDIAG(ad[j].x), at[0][j & 1]); at[1][j & 1] = mfma(Wt[j], BDIAG(ad[j].y), at[1][j & 1]); }
            if (role_w2) {
#pragma unroll
                for (int j = 8; j < 12; ++j) { const v2u a2 = *(const v2u*)&act[(8 * (sw + 4 * j) + lrow) * 2]; at[0][j & 1] = mfma(Wt[j], BDIAG(a2.x), at[0][j & 1]); at[1][j & 1] = mfma(Wt[j], BDIAG(a2.y), at[1][j & 1]); }
            }
            at[0][0] += at[0][1]; at[1][0] += at[1][1];
            float* red2 = (float*)(lds + L_RED2);
            if ((lane >> 5) == ((lane >> 3) & 1)) {
                const int r1 = lane >> 5, cb = 32 * ((lane >> 4) & 1) + (lane & 7);
#pragma unroll
                for (int bb = 0; bb < 2; ++bb)
#pragma unroll
                    for (int r = 0; r < 4; ++r) red2[((sw * 2 + r1) * 2 + bb) * 64 + cb + 8 * r] = at[bb][0][r];
            }
            asm volatile("s_waitcnt lgkmcnt(0)" ::: "memory");
            unsigned arr = 0;
            if (lane == 0) arr = __hip_atomic_fetch_add((LAS unsigned*)(qflag + 4), 1u, __ATOMIC_RELAXED, __HIP_MEMORY_SCOPE_WORKGROUP);
            arr = __builtin_amdgcn_readfirstlane(arr);
            asm volatile("" ::: "memory");
            if (arr == 3u) {
                const int bb = lane >> 5, pr = lane & 31;
                float v0 = 0.f, v1 = 0.f;
#pragma unroll
                for (int i = 0; i < 8; ++i) { const float2 t = *(const float2*)&red2[(i * 2 + bb) * 64 + 2 * pr]; v0 += t.x; v1 += t.y; }
                const int off = role_wo ? WS_SP + (((tcg * 2 + bb) * 16 + trg) * 32 + pr) * 16 : WS_MP + (((tcg * 2 + bb) * 12 + trg) * 32 + pr) * 16;
                STSC(mkpair(v0, v1, T), rws, off);
            }
        }
    }
    STAMPW(9, w == 0);

    if (bid >= 144 && bid < 216 && w >= 1 && w <= NFIN) {
        if (w == 2) __builtin_amdgcn_s_sleep(STAG); if (w == 3) __builtin_amdgcn_s_sleep(STAG / 2);
        const int u = bid - 144, cg = u >> 3, fb = (u >> 2) & 1, q4 = u & 3;
        const int p8 = lane >> 3, j = lane & 7;
        const int col = 64 * cg + 16 * q4 + 2 * j;
        const unsigned xw = *(const unsigned*)(x + fb * D + col), bw = *(const unsigned*)(b2 + col);
        const int offs = WS_SP + (((cg * 2 + fb) * 16 + p8) * 32 + 8 * q4 + j) * 16;
        const int offm = WS_MP + (((cg * 2 + fb) * 12 + p8) * 32 + 8 * q4 + j) * 16;
        v4u g[4]; bool ok; unsigned spin = 0;
        do {
            g[0] = LDSC(rws, offs); g[1] = LDSC(rws, offs + 8 * 32 * 16); g[2] = LDSC(rws, offm); g[3] = LDSC(rws, p8 < 4 ? offm + 8 * 32 * 16 : OOB);
            ok = tag_ok(g[0], T) & tag_ok(g[1], T) & tag_ok(g[2], T) & ((p8 >= 4) | tag_ok(g[3], T));
            if (__all(ok)) break;
            SPIN_PAUSE(1);
        } while (++spin < SPIN_CAP);
        float s0 = __uint_as_float(g[0].x) + __uint_as_float(g[1].x), s1 = __uint_as_float(g[0].z) + __uint_as_float(g[1].z);
        float m0 = __uint_as_float(g[2].x) + __uint_as_float(g[3].x), m1 = __uint_as_float(g[2].z) + __uint_as_float(g[3].z);
        s0 += dpp<0x128>(s0); s1 += dpp<0x128>(s1); m0 += dpp<0x128>(m0); m1 += dpp<0x128>(m1);
        s0 = xrow_sum(s0); s1 = xrow_sum(s1); m0 = xrow_sum(m0); m1 = xrow_sum(m1);
        if (lane < 8) {
            const float mv0 = rbf(rbf(m0) + bf2f(bw & 0xffffu)), mv1 = rbf(rbf(m1) + __uint_as_float(bw & 0xffff0000u));
            const float g0 = rbf(mv0 / (1.f + __expf(-mv0))), g1 = rbf(mv1 / (1.f + __expf(-mv1)));
            *(unsigned*)(out + fb * D + col) = cvtpk(bf2f(xw & 0xffffu) + rbf(rbf(s0) * g0), __uint_as_float(xw & 0xffff0000u) + rbf(rbf(s1) * g1));
        }
        if (u == 0 && lane == 0 && w == 1) __builtin_amdgcn_raw_buffer_store_b32(T, rws, WS_E, 0, 16);
        STAMPW(14, true);
    }
    STAMPW(14, w == 0);
}

extern "C" void kernel_launch(void* const* d_in, const int* in_sizes, int n_in, void* d_out, int out_size, void* d_ws, size_t ws_size, hipStream_t stream) {
    fused_kernel<<<256, 768, 0, stream>>>((const u16*)d_in[0], (const u16*)d_in[1], (const u16*)d_in[2], (const u16*)d_in[3], (const u16*)d_in[4],
                                          (const u16*)d_in[6], (const u16*)d_in[7], (const u16*)d_in[8], (const u16*)d_in[9], (u16*)d_out, (unsigned char*)d_ws);
}
```

```cpp
#include <hip/hip_runtime.h>
#include <stdint.h>

typedef unsigned short u16;
typedef unsigned v4u __attribute__((ext_vector_type(4)));
typedef float f32x4 __attribute__((ext_vector_type(4)));
typedef short bf16x8 __attribute__((ext_vector_type(8)));
typedef __bf16 bf16x2 __attribute__((ext_vector_type(2)));
#define LAS __attribute__((address_space(3)))
#define STAG 14
#define NFIN 1
#define HSLEEP 2
#define CODEPF 16
#define B0MODE 0

constexpr int D = 576, HKV = 16, SK = 16384, DH = 128, WIN = 4096, F = 4608;
constexpr int WS_E = 0;
constexpr int WS_QP = 4096;
constexpr int WS_HP = WS_QP + 16 * 2 * 4 * 128 * 16;
constexpr int WS_AP = WS_HP + 72 * 2 * 3 * 32 * 16;
constexpr int AP_REC = 136 * 16;
constexpr int WS_SP = WS_AP + 256 * AP_REC;
constexpr int WS_MP = WS_SP + 9 * 2 * 16 * 32 * 16;
constexpr int WS_END = WS_MP + 9 * 2 * 12 * 32 * 16;
constexpr int WS_STAMP = (WS_END + 4095) / 4096 * 4096;
constexpr unsigned SPIN_CAP = 1u << 18;
constexpr int OOB = 0x7ffffff0;

#define RSRC(p, n) __builtin_amdgcn_make_buffer_rsrc((void*)(p), 0, (n), 0x00020000)
#define LDNT(rs, off) __builtin_amdgcn_raw_buffer_load_b128(rs, off, 0, 2)
#define LDSC(rs, off) __builtin_amdgcn_raw_buffer_load_b128(rs, off, 0, 16)
#define STSC(v, rs, off) __builtin_amdgcn_raw_buffer_store_b128(v, rs, off, 0, 16)

__device__ __forceinline__ float bf2f(unsigned b) { return __uint_as_float(b << 16); }
typedef float f32x2 __attribute__((ext_vector_type(2)));
__device__ __forceinline__ unsigned cvtpk(float lo, float hi) { const f32x2 v = {lo, hi}; return __builtin_bit_cast(unsigned, __builtin_convertvector(v, bf16x2)); }
__device__ __forceinline__ float rbf(float f) { return __uint_as_float(cvtpk(f, f) << 16); }
__device__ __forceinline__ float dot2(unsigned k, unsigned q, float acc) {
    return __builtin_amdgcn_fdot2_f32_bf16(__builtin_bit_cast(bf16x2, k), __builtin_bit_cast(bf16x2, q), acc, false);
}
template <int CTRL> __device__ __forceinline__ float dpp(float x) {
    return __builtin_bit_cast(float, __builtin_amdgcn_mov_dpp(__builtin_bit_cast(int, x), CTRL, 0xf, 0xf, true));
}
__device__ __forceinline__ f32x4 mfma(v4u a, v4u b, f32x4 c) {
    return __builtin_amdgcn_mfma_f32_16x16x32_bf16(__builtin_bit_cast(bf16x8, a), __builtin_bit_cast(bf16x8, b), c, 0, 0, 0);
}
__device__ __forceinline__ float xrow_sum(float x) {
    auto s = __builtin_amdgcn_permlane16_swap(__float_as_uint(x), __float_as_uint(x), false, false);
    x = __uint_as_float(s[0]) + __uint_as_float(s[1]);
    auto t = __builtin_amdgcn_permlane32_swap(__float_as_uint(x), __float_as_uint(x), false, false);
    return __uint_as_float(t[0]) + __uint_as_float(t[1]);
}
__device__ __forceinline__ float xrow_max(float x) {
    auto s = __builtin_amdgcn_permlane16_swap(__float_as_uint(x), __float_as_uint(x), false, false);
    x = fmaxf(__uint_as_float(s[0]), __uint_as_float(s[1]));
    auto t = __builtin_amdgcn_permlane32_swap(__float_as_uint(x), __float_as_uint(x), false, false);
    return fmaxf(__uint_as_float(t[0]), __uint_as_float(t[1]));
}
__device__ __forceinline__ float wave_sum(float v) {
    v += dpp<0xB1>(v); v += dpp<0x4E>(v); v += dpp<0x141>(v); v += dpp<0x140>(v);
    return xrow_sum(v);
}
__device__ __forceinline__ bool tag_ok(v4u p, unsigned T) { return (p.y == T) & (p.w == ~T); }
__device__ __forceinline__ v4u mkpair(float a, float b, unsigned T) { return v4u{__float_as_uint(a), T, __float_as_uint(b), ~T}; }

#define SPIN_PAUSE(n) do { __builtin_amdgcn_s_sleep(n); asm volatile("" :: "s"(ws) : "memory"); } while (0)
#define STAMPW(i, cond) do { } while (0)

constexpr int L_XS = 0;
constexpr int L_Q = 4608;
constexpr int L_ACT = 13312 + 8192;
constexpr int L_ML = 6656;
constexpr int L_GML = 6784;
constexpr int L_FLAG = 7040;
constexpr int L_SCR = 7168;
constexpr int L_RED2 = 9216;
constexpr int L_RED = 13312;
constexpr int L_K = L_RED + 16384;
constexpr int L_BYTES = L_K + 131072;

__global__ void __launch_bounds__(768, 3)
fused_kernel(const u16* __restrict__ x, const u16* __restrict__ kc, const u16* __restrict__ vc, const u16* __restrict__ wq, const u16* __restrict__ wo,
             const u16* __restrict__ w1, const u16* __restrict__ b1, const u16* __restrict__ w2, const u16* __restrict__ b2,
             u16* __restrict__ out, unsigned char* ws) {
    __shared__ __attribute__((aligned(16))) unsigned char lds[L_BYTES];
    const int tid = threadIdx.x, lane = tid & 63, w = __builtin_amdgcn_readfirstlane(tid >> 6), bid = blockIdx.x;
    unsigned long long pc0; asm volatile("s_getpc_b64 %0" : "=s"(pc0));
    STAMPW(0, w == 0);
    const int s = bid & 7, b = (bid >> 3) & 1, kvh = bid >> 4;
    const int kq = (bid & 15) >> 2, cq = bid & 3;
    const bool has_w1 = bid < 216;
    const int c1 = bid % 72, k1 = bid / 72;
    const bool role_wo = bid < 144, role_w2 = bid >= 144 && bid < 252;
    const int u2 = bid - 144;
    const int tcg = role_wo ? bid % 9 : u2 % 9;
    const int trg = role_wo ? bid / 9 : u2 / 9;
    const int trows = role_wo ? 256 : 384;
    const __amdgpu_buffer_rsrc_t rws = RSRC(ws, WS_END);
    volatile LAS unsigned* qflag = (volatile LAS unsigned*)(LAS unsigned char*)(lds + L_FLAG);
    if (tid < 8) qflag[tid] = 0u;

    const int lrow = lane >> 3, lcol = (lane & 7) * 16;
    const unsigned hm = (lane & 1) ? 0xffff0000u : 0x0000ffffu;
    const int dsel = (lane >> 1) & 3;
    const unsigned M0 = dsel == 0 ? hm : 0u, M1 = dsel == 1 ? hm : 0u, M2 = dsel == 2 ? hm : 0u, M3 = dsel == 3 ? hm : 0u;
#define BDIAG(dup) v4u{(dup) & M0, (dup) & M1, (dup) & M2, (dup) & M3}
    unsigned T;

    v4u xr[3], Wf[24];
    if (w == 8 || w == 9) {
        __builtin_amdgcn_s_setprio(3);
        const __amdgpu_buffer_rsrc_t rx = RSRC(x, 2 * D * 2);
        const __amdgpu_buffer_rsrc_t rw = w == 8 ? RSRC(wq + (size_t)(144 * kq) * 4096 + 256 * kvh + 64 * cq, 144 * 4096 * 2 - 256 * kvh * 2 - 128 * cq)
                                                 : RSRC(w1 + (size_t)(192 * k1) * F + 64 * c1, has_w1 ? 192 * F * 2 - 128 * c1 : 0);
        const int ld = w == 8 ? 4096 * 2 : F * 2;
        const int nld = w == 8 ? 18 : 24;
#pragma unroll
        for (int j = 0; j < 3; ++j) xr[j] = __builtin_amdgcn_raw_buffer_load_b128(rx, (lane + 64 * j) * 16, 0, 0);
#pragma unroll
        for (int j = 0; j < 24; ++j) Wf[j] = LDNT(rw, j < nld ? (8 * j + lrow) * ld + lcol : OOB);
    }
    if (w >= 10) {
        const __amdgpu_buffer_rsrc_t rc = RSRC(pc0 & ~255ull, CODEPF * 1024);
#pragma unroll
        for (int i = 0; i < CODEPF / 2; ++i) Wf[i] = __builtin_amdgcn_raw_buffer_load_b128(rc, ((w - 10) * (CODEPF / 2) + i) * 1024 + lane * 16, 0, 0);
    }
    unsigned ep = 0;
    if (w == 8 || w == 9) ep = __builtin_amdgcn_raw_buffer_load_b32(rws, WS_E, 0, 16);
    __builtin_amdgcn_sched_barrier(0);
    __syncthreads();
    __builtin_amdgcn_sched_barrier(0);

    if (__builtin_expect(w < 8, 1)) {
        const __amdgpu_buffer_rsrc_t rk = RSRC(kc + ((size_t)(b * HKV + kvh) * SK + (SK - WIN) + s * 512 + w * 64) * DH, 64 * 256);
        const __amdgpu_buffer_rsrc_t rv = RSRC(vc + ((size_t)(b * HKV + kvh) * SK + (SK - WIN) + s * 512 + w * 64) * DH, 64 * 256);
        v4u K[16], V[16];
        __builtin_amdgcn_s_setprio(1);
#pragma unroll
        for (int j = 0; j < 16; ++j) K[j] = LDNT(rk, j * 1024 + lane * 16);
        __builtin_amdgcn_sched_barrier(0);
        __builtin_amdgcn_s_setprio(0);
        __builtin_amdgcn_sched_barrier(0);
#pragma unroll
        for (int j = 0; j < 16; ++j) V[j] = LDNT(rv, j * 1024 + lane * 16);
        __builtin_amdgcn_sched_barrier(0);
        if (lane == 0) __hip_atomic_fetch_add((LAS unsigned*)(qflag + 5), 1u, __ATOMIC_RELAXED, __HIP_MEMORY_SCOPE_WORKGROUP);
        STAMPW(1, w == 0); STAMPW(2, w == 7);
        const int g = lane >> 4, c = lane & 15;
#pragma unroll
        for (int j = 0; j < 16; ++j) { const int i = 4 * (j & 3) + g; *(v4u*)(lds + L_K + w * 16384 + (j >> 2) * 4096 + i * 256 + ((c ^ (4 * g + (j & 3))) * 16)) = K[j]; }
        { unsigned spin = 0; while (*qflag == 0u && ++spin < (SPIN_CAP << 4)) __builtin_amdgcn_s_sleep(1); T = qflag[1]; }
        asm volatile("s_waitcnt lgkmcnt(0)" ::: "memory");
        STAMPW(3, w == 0); STAMPW(4, w == 7);
        v4u qf[4];
#pragma unroll
        for (int ks = 0; ks < 4; ++ks) qf[ks] = *(const v4u*)(lds + L_Q + (c >> 3) * 256 + (4 * ks + g) * 16);
        f32x4 sc[4];
#pragma unroll
        for (int t = 0; t < 4; ++t) {
            sc[t] = f32x4{0, 0, 0, 0};
#pragma unroll
            for (int ks = 0; ks < 4; ++ks) {
                const v4u ka = *(const v4u*)(lds + L_K + w * 16384 + t * 4096 + (4 * (c & 3) + (c >> 2)) * 256 + (((4 * ks + g) ^ c) * 16));
                sc[t] = mfma(ka, qf[ks], sc[t]);
            }
        }
        float m = -INFINITY;
#pragma unroll
        for (int t = 0; t < 4; ++t)
#pragma unroll
            for (int r = 0; r < 4; ++r) { sc[t][r] = rbf(rbf(sc[t][r]) * 0.088388347648318440f); m = fmaxf(m, sc[t][r]); }
        m = xrow_max(m);
        float l = 0.f;
        constexpr float LOG2E = 1.4426950408889634f;
        const float mm = m * LOG2E;
        unsigned pd[16];
#pragma unroll
        for (int t = 0; t < 4; ++t)
#pragma unroll
            for (int r = 0; r < 4; ++r) { const float p = __builtin_amdgcn_exp2f(sc[t][r] * LOG2E - mm); l += p; pd[4 * t + r] = cvtpk(p, p); }
        l = xrow_sum(l);
        STAMPW(5, w == 7);
        f32x4 acc0 = {0, 0, 0, 0}, acc1 = {0, 0, 0, 0};
#pragma unroll
        for (int t = 0; t < 16; t += 2) { acc0 = mfma(V[t], BDIAG(pd[t]), acc0); acc1 = mfma(V[t + 1], BDIAG(pd[t + 1]), acc1); }
        acc0 += acc1;
        __builtin_amdgcn_s_setprio(3);
        const int hh = (lane >> 3) & 1;
        *(f32x4*)(lds + L_RED + (w * 64 + lane) * 16) = acc0;
        if ((lane & 0x37) == 0) *(float2*)(lds + L_ML + (w * 2 + hh) * 8) = make_float2(mm, l);
        asm volatile("s_waitcnt lgkmcnt(0)" ::: "memory");
        unsigned arr = 0;
        if (lane == 0) arr = __hip_atomic_fetch_add((LAS unsigned*)(qflag + 2), 1u, __ATOMIC_RELAXED, __HIP_MEMORY_SCOPE_WORKGROUP);
        arr = __builtin_amdgcn_readfirstlane(arr);
        asm volatile("" ::: "memory");
        if (arr == 7u) {
            float2 ml[8]; f32x4 pa[8];
#pragma unroll
            for (int i = 0; i < 8; ++i) { ml[i] = *(const float2*)(lds + L_ML + (i * 2 + hh) * 8); pa[i] = *(const f32x4*)(lds + L_RED + (i * 64 + lane) * 16); }
            float M = ml[0].x;
#pragma unroll
            for (int i = 1; i < 8; ++i) M = fmaxf(M, ml[i].x);
            f32x4 a = {0, 0, 0, 0}; float L = 0.f;
#pragma unroll
            for (int i = 0; i < 8; ++i) { const float wt = __builtin_amdgcn_exp2f(ml[i].x - M); a += wt * pa[i]; L += wt * ml[i].y; }
            const int off = WS_AP + bid * AP_REC + lane * 16;
            STSC(mkpair(a[0], a[1], T), rws, off);
            STSC(mkpair(a[2], a[3], T), rws, off + 1024);
            STSC(mkpair(M, L, T), rws, (lane & 0x37) == 0 ? WS_AP + bid * AP_REC + (128 + hh) * 16 : OOB);
            if (lane == 0) qflag[6] = 1u;
        }
        if (role_wo && w < 4) {
            { unsigned spin = 0; while (qflag[6] == 0u && ++spin < (SPIN_CAP << 4)) __builtin_amdgcn_s_sleep(2); }
            const int bb = (w >> 1) & 1, h2 = w & 1;
            const unsigned bit = 1u << (w & 3);
            const int base = WS_AP + (trg * 16 + bb * 8) * AP_REC;
            v4u g[8], gm[8];
            bool ok = false;
            unsigned spin = 0;
            do {
                if (qflag[3] & bit) break;
#pragma unroll
                for (int i = 0; i < 8; ++i) { g[i] = LDSC(rws, base + i * AP_REC + ((lane >> 5) * 64 + ((lane >> 3) & 3) * 16 + h2 * 8 + (lane & 7)) * 16); gm[i] = LDSC(rws, base + i * AP_REC + (128 + h2) * 16); }
                bool o = true;
#pragma unroll
                for (int i = 0; i < 8; ++i) o &= tag_ok(g[i], T) & tag_ok(gm[i], T);
                ok = __all(o);
                if (ok) break;
                SPIN_PAUSE(HSLEEP);
            } while (++spin < SPIN_CAP);
            if (ok) {
                float M = __uint_as_float(gm[0].x);
#pragma unroll
                for (int i = 1; i < 8; ++i) M = fmaxf(M, __uint_as_float(gm[i].x));
                float a0 = 0.f, a1 = 0.f, L = 0.f;
#pragma unroll
                for (int i = 0; i < 8; ++i) {
                    const float wt = __builtin_amdgcn_exp2f(__uint_as_float(gm[i].x) - M);
                    a0 += wt * __uint_as_float(g[i].x); a1 += wt * __uint_as_float(g[i].z); L += wt * __uint_as_float(gm[i].z);
                }
                const float ri = 1.f / L;
                const int dl = h2 * 128 + 32 * ((lane >> 3) & 3) + 16 * (lane >> 5) + (lane & 7);
                unsigned* act = (unsigned*)(lds + L_ACT);
                act[dl * 2 + bb] = cvtpk(a0 * ri, a0 * ri); act[(dl + 8) * 2 + bb] = cvtpk(a1 * ri, a1 * ri);
                asm volatile("s_waitcnt lgkmcnt(0)" ::: "memory");
                if (lane == 0) __hip_atomic_fetch_or((LAS unsigned*)(qflag + 3), bit, __ATOMIC_RELAXED, __HIP_MEMORY_SCOPE_WORKGROUP);
            } else if (spin >= SPIN_CAP) {
                if (lane == 0) __hip_atomic_fetch_or((LAS unsigned*)(qflag + 3), bit, __ATOMIC_RELAXED, __HIP_MEMORY_SCOPE_WORKGROUP);
            }
        }
    } else {
        const int sw = w - 8;
        if (w >= 10) {
#pragma unroll
            for (int i = 0; i < CODEPF / 2; ++i) asm volatile("" :: "v"(Wf[i]));
        }
        const __amdgpu_buffer_rsrc_t rt = RSRC(role_wo ? wo : w2, role_wo ? 4096 * D * 2 : (role_w2 ? F * D * 2 : 0));
        if (sw < 2) {
            const int row0 = sw == 0 ? 144 * kq : 192 * k1;
            unsigned char* xsb = lds + L_XS + sw * 2304;
            {
                float xv[3][8];
#pragma unroll
                for (int j = 0; j < 3; ++j)
#pragma unroll
                    for (int k = 0; k < 4; ++k) { xv[j][2 * k] = bf2f(xr[j][k] & 0xffffu); xv[j][2 * k + 1] = __uint_as_float(xr[j][k] & 0xffff0000u); }
                float sj[3];
#pragma unroll
                for (int j = 0; j < 3; ++j) { float t = 0.f;
#pragma unroll
                    for (int k = 0; k < 8; ++k) t += xv[j][k];
                    sj[j] = t; }
                const bool c1b0 = lane < 8, c2ok = lane < 16;
                const float mean0 = wave_sum(sj[0] + (c1b0 ? sj[1] : 0.f)) * (1.f / D);
                const float mean1 = wave_sum((c1b0 ? 0.f : sj[1]) + sj[2]) * (1.f / D);
                const float mj[3] = {mean0, c1b0 ? mean0 : mean1, mean1};
#pragma unroll
                for (int j = 0; j < 3; ++j) { float t = 0.f;
#pragma unroll
                    for (int k = 0; k < 8; ++k) { const float d = xv[j][k] - mj[j]; t += d * d; }
                    sj[j] = t; }
                if (!c2ok) sj[2] = 0.f;
                const float var0 = wave_sum(sj[0] + (c1b0 ? sj[1] : 0.f)) * (1.f / D);
                const float var1 = wave_sum((c1b0 ? 0.f : sj[1]) + sj[2]) * (1.f / D);
                const float mb0 = rbf(mean0), mb1 = rbf(mean1);
                const float den0 = rbf(sqrtf(rbf(rbf(var0) + 1e-5f))), den1 = rbf(sqrtf(rbf(rbf(var1) + 1e-5f)));
                const float ri0 = 1.f / den0, ri1 = 1.f / den1;
                const float mbj[3] = {mb0, c1b0 ? mb0 : mb1, mb1}, dnj[3] = {ri0, c1b0 ? ri0 : ri1, ri1};
#pragma unroll
                for (int j = 0; j < 3; ++j) {
                    v4u o;
#pragma unroll
                    for (int k = 0; k < 4; ++k) o[k] = cvtpk(rbf(xv[j][2 * k] - mbj[j]) * dnj[j], rbf(xv[j][2 * k + 1] - mbj[j]) * dnj[j]);
                    if (j < 2 || c2ok) *(v4u*)(xsb + (lane + 64 * j) * 16) = o;
                }
            }
            __builtin_amdgcn_fence(__ATOMIC_ACQ_REL, "wavefront");
            asm volatile("" ::: "memory");
            STAMPW(10, sw == 0);
            const u16* xs = (const u16*)xsb;
            f32x4 af[2] = {{0, 0, 0, 0}, {0, 0, 0, 0}}, ag[2] = {{0, 0, 0, 0}, {0, 0, 0, 0}};
#pragma unroll
            for (int j = 0; j < 18; ++j) {
                const int r = min(row0 + 8 * j + lrow, D - 1);
#pragma unroll
                for (int bb = 0; bb < 2; ++bb) { const unsigned v = xs[bb * D + r]; const unsigned dup = v | (v << 16);
                    if (j & 1) ag[bb] = mfma(Wf[j], BDIAG(dup), ag[bb]); else af[bb] = mfma(Wf[j], BDIAG(dup), af[bb]); }
            }
            if (sw == 1) {
#pragma unroll
                for (int j = 18; j < 24; ++j) {
                    const int r = min(row0 + 8 * j + lrow, D - 1);
#pragma unroll
                    for (int bb = 0; bb < 2; ++bb) { const unsigned v = xs[bb * D + r]; const unsigned dup = v | (v << 16);
                        if (j & 1) ag[bb] = mfma(Wf[j], BDIAG(dup), ag[bb]); else af[bb] = mfma(Wf[j], BDIAG(dup), af[bb]); }
                }
            }
            af[0] += ag[0]; af[1] += ag[1];
            T = __builtin_amdgcn_readfirstlane(ep) + 1u;
            float* scr = (float*)(lds + L_SCR + sw * 1024);
            if ((lane >> 5) == ((lane >> 3) & 1)) {
                const int r1 = lane >> 5, cb = 32 * ((lane >> 4) & 1) + (lane & 7);
#pragma unroll
                for (int bb = 0; bb < 2; ++bb)
#pragma unroll
                    for (int r = 0; r < 4; ++r) scr[(r1 * 2 + bb) * 64 + cb + 8 * r] = af[bb][r];
            }
            __builtin_amdgcn_fence(__ATOMIC_ACQ_REL, "wavefront");
            asm volatile("" ::: "memory");
            {
                const int bb = lane >> 5, pr = lane & 31;
                const float2 t0 = *(const float2*)&scr[bb * 64 + 2 * pr], t1 = *(const float2*)&scr[(2 + bb) * 64 + 2 * pr];
                const int off = sw == 0 ? WS_QP + (((kvh * 2 + bb) * 4 + kq) * 128 + cq * 32 + pr) * 16
                                        : (has_w1 ? WS_HP + (((c1 * 2 + bb) * 3 + k1) * 32 + pr) * 16 : OOB);
                STSC(mkpair(t0.x + t1.x, t0.y + t1.y, T), rws, off);
            }
            STAMPW(11, sw == 0);
            if (sw == 0) {
                const int qoff = WS_QP + ((kvh * 2 + b) * 4 * 128 + lane) * 16;
                v4u gq[8]; bool ok; unsigned spin = 0;
                do {
#pragma unroll
                    for (int i = 0; i < 8; ++i) gq[i] = LDSC(rws, qoff + (i >> 1) * 128 * 16 + (i & 1) * 64 * 16);
                    ok = true;
#pragma unroll
                    for (int i = 0; i < 8; ++i) ok &= tag_ok(gq[i], T);
                    if (__all(ok)) break;
                    SPIN_PAUSE(1);
                } while (++spin < SPIN_CAP);
#pragma unroll
                for (int hf2 = 0; hf2 < 2; ++hf2) {
                    const float q0 = (__uint_as_float(gq[hf2].x) + __uint_as_float(gq[2 + hf2].x)) + (__uint_as_float(gq[4 + hf2].x) + __uint_as_float(gq[6 + hf2].x));
                    const float q1 = (__uint_as_float(gq[hf2].z) + __uint_as_float(gq[2 + hf2].z)) + (__uint_as_float(gq[4 + hf2].z) + __uint_as_float(gq[6 + hf2].z));
                    ((unsigned*)(lds + L_Q))[hf2 * 64 + lane] = cvtpk(q0, q1);
                }
                if (lane == 0) qflag[1] = T;
                asm volatile("s_waitcnt lgkmcnt(0)" ::: "memory");
                if (lane == 0) *qflag = 1u;
                STAMPW(12, true);
            }
            __builtin_amdgcn_s_setprio(0);
        }
        __builtin_amdgcn_sched_barrier(0);
        { unsigned spin = 0; while (qflag[5] < 8u && ++spin < (SPIN_CAP << 4)) __builtin_amdgcn_s_sleep(2); }
        v4u Wt[12];
#pragma unroll
        for (int j = 0; j < 12; ++j) { const int u = sw + 4 * j; Wt[j] = LDNT(rt, (8 * u < trows) ? ((trows * trg + 8 * u + lrow) * D + 64 * tcg) * 2 + lcol : OOB); }
        __builtin_amdgcn_sched_barrier(0);
        STAMPW(13, sw == 3);
        if (sw >= 2) {
            unsigned spin = 0; while (*qflag == 0u && ++spin < (SPIN_CAP << 4)) __builtin_amdgcn_s_sleep(4);
            T = qflag[1];
        }
        if (role_w2) {
            const int st = tid - 512;
            const __amdgpu_buffer_rsrc_t rb1 = RSRC(b1, F * 2);
            v4u gh[2][3]; unsigned b1v[2]; int hoff[2];
#pragma unroll
            for (int e = 0; e < 2; ++e) {
                const int it = st + 256 * e; const bool valid = it < 384;
                const int hbb = it / 192, hpr = it % 192, hf = 384 * trg + 2 * hpr;
                hoff[e] = valid ? WS_HP + ((((hf >> 6) * 2 + hbb) * 3) * 32 + ((hf & 63) >> 1)) * 16 : OOB;
                b1v[e] = __builtin_amdgcn_raw_buffer_load_b32(rb1, valid ? hf * 2 : OOB, 0, 0);
            }
            bool ok; unsigned spin = 0;
            do {
                ok = true;
#pragma unroll
                for (int e = 0; e < 2; ++e)
#pragma unroll
                    for (int i = 0; i < 3; ++i) { gh[e][i] = LDSC(rws, hoff[e] == OOB ? OOB : hoff[e] + i * 32 * 16); }
#pragma unroll
                for (int e = 0; e < 2; ++e)
#pragma unroll
                    for (int i = 0; i < 3; ++i) ok &= (hoff[e] == OOB) | tag_ok(gh[e][i], T);
                if (__all(ok)) break;
                SPIN_PAUSE(2);
            } while (++spin < SPIN_CAP);
#pragma unroll
            for (int e = 0; e < 2; ++e) {
                const int it = st + 256 * e;
                float h0 = __uint_as_float(gh[e][0].x) + __uint_as_float(gh[e][1].x) + __uint_as_float(gh[e][2].x);
                float h1 = __uint_as_float(gh[e][0].z) + __uint_as_float(gh[e][1].z) + __uint_as_float(gh[e][2].z);
                h0 = rbf(rbf(h0) + bf2f(b1v[e] & 0xffffu)); h1 = rbf(rbf(h1) + __uint_as_float(b1v[e] & 0xffff0000u));
                h0 = h0 > 0.f ? h0 : 0.f; h1 = h1 > 0.f ? h1 : 0.f;
                h0 *= h0; h1 *= h1;
                const int hbb = it / 192, hpr = it % 192;
                if (it < 384) { unsigned* act = (unsigned*)(lds + L_ACT); act[(2 * hpr) * 2 + hbb] = cvtpk(h0, h0); act[(2 * hpr + 1) * 2 + hbb] = cvtpk(h1, h1); }
            }
            asm volatile("s_waitcnt lgkmcnt(0)" ::: "memory");
            if (lane == 0) __hip_atomic_fetch_or((LAS unsigned*)(qflag + 3), 1u << sw, __ATOMIC_RELAXED, __HIP_MEMORY_SCOPE_WORKGROUP);
        }
        if (role_wo || role_w2) {
            { unsigned spin = 0; while (qflag[3] != 15u && ++spin < (SPIN_CAP << 4)) __builtin_amdgcn_s_sleep(0); }
            asm volatile("" ::: "memory");
            const unsigned* act = (const unsigned*)(lds + L_ACT);
            f32x4 at[2][2] = {{{0, 0, 0, 0}, {0, 0, 0, 0}}, {{0, 0, 0, 0}, {0, 0, 0, 0}}};
            typedef unsigned v2u __attribute__((ext_vector_type(2)));
            v2u ad[8];
#pragma unroll
            for (int j = 0; j < 8; ++j) ad[j] = *(const v2u*)&act[(8 * (sw + 4 * j) + lrow) * 2];
#pragma unroll
            for (int j = 0; j < 8; ++j) { at[0][j & 1] = mfma(Wt[j], BDIAG(ad[j].x), at[0][j & 1]); at[1][j & 1] = mfma(Wt[j], BDIAG(ad[j].y), at[1][j & 1]); }
            if (role_w2) {
#pragma unroll
                for (int j = 8; j < 12; ++j) { const v2u a2 = *(const v2u*)&act[(8 * (sw + 4 * j) + lrow) * 2]; at[0][j & 1] = mfma(Wt[j], BDIAG(a2.x), at[0][j & 1]); at[1][j & 1] = mfma(Wt[j], BDIAG(a2.y), at[1][j & 1]); }
            }
            at[0][0] += at[0][1]; at[1][0] += at[1][1];
            float* red2 = (float*)(lds + L_RED2);
            if ((lane >> 5) == ((lane >> 3) & 1)) {
                const int r1 = lane >> 5, cb = 32 * ((lane >> 4) & 1) + (lane & 7);
#pragma unroll
                for (int bb = 0; bb < 2; ++bb)
#pragma unroll
                    for (int r = 0; r < 4; ++r) red2[((sw * 2 + r1) * 2 + bb) * 64 + cb + 8 * r] = at[bb][0][r];
            }
            asm volatile("s_waitcnt lgkmcnt(0)" ::: "memory");
            unsigned arr = 0;
            if (lane == 0) arr = __hip_atomic_fetch_add((LAS unsigned*)(qflag + 4), 1u, __ATOMIC_RELAXED, __HIP_MEMORY_SCOPE_WORKGROUP);
            arr = __builtin_amdgcn_readfirstlane(arr);
            asm volatile("" ::: "memory");
            if (arr == 3u) {
                const int bb = lane >> 5, pr = lane & 31;
                float v0 = 0.f, v1 = 0.f;
#pragma unroll
                for (int i = 0; i < 8; ++i) { const float2 t = *(const float2*)&red2[(i * 2 + bb) * 64 + 2 * pr]; v0 += t.x; v1 += t.y; }
                const int off = role_wo ? WS_SP + (((tcg * 2 + bb) * 16 + trg) * 32 + pr) * 16 : WS_MP + (((tcg * 2 + bb) * 12 + trg) * 32 + pr) * 16;
                STSC(mkpair(v0, v1, T), rws, off);
            }
        }
    }
    STAMPW(9, w == 0);

    if (bid >= 144 && bid < 216 && w >= 1 && w <= NFIN) {
        if (w == 2) __builtin_amdgcn_s_sleep(STAG); if (w == 3) __builtin_amdgcn_s_sleep(STAG / 2);
        const int u = bid - 144, cg = u >> 3, fb = (u >> 2) & 1, q4 = u & 3;
        const int p8 = lane >> 3, j = lane & 7;
        const int col = 64 * cg + 16 * q4 + 2 * j;
        const unsigned xw = *(const unsigned*)(x + fb * D + col), bw = *(const unsigned*)(b2 + col);
        const int offs = WS_SP + (((cg * 2 + fb) * 16 + p8) * 32 + 8 * q4 + j) * 16;
        const int offm = WS_MP + (((cg * 2 + fb) * 12 + p8) * 32 + 8 * q4 + j) * 16;
        v4u g[4]; bool ok; unsigned spin = 0;
        do {
            g[0] = LDSC(rws, offs); g[1] = LDSC(rws, offs + 8 * 32 * 16); g[2] = LDSC(rws, offm); g[3] = LDSC(rws, p8 < 4 ? offm + 8 * 32 * 16 : OOB);
            ok = tag_ok(g[0], T) & tag_ok(g[1], T) & tag_ok(g[2], T) & ((p8 >= 4) | tag_ok(g[3], T));
            if (__all(ok)) break;
            SPIN_PAUSE(1);
        } while (++spin < SPIN_CAP);
        float s0 = __uint_as_float(g[0].x) + __uint_as_float(g[1].x), s1 = __uint_as_float(g[0].z) + __uint_as_float(g[1].z);
        float m0 = __uint_as_float(g[2].x) + __uint_as_float(g[3].x), m1 = __uint_as_float(g[2].z) + __uint_as_float(g[3].z);
        s0 += dpp<0x128>(s0); s1 += dpp<0x128>(s1); m0 += dpp<0x128>(m0); m1 += dpp<0x128>(m1);
        s0 = xrow_sum(s0); s1 = xrow_sum(s1); m0 = xrow_sum(m0); m1 = xrow_sum(m1);
        if (lane < 8) {
            const float mv0 = rbf(rbf(m0) + bf2f(bw & 0xffffu)), mv1 = rbf(rbf(m1) + __uint_as_float(bw & 0xffff0000u));
            const float g0 = rbf(mv0 / (1.f + __expf(-mv0))), g1 = rbf(mv1 / (1.f + __expf(-mv1)));
            *(unsigned*)(out + fb * D + col) = cvtpk(bf2f(xw & 0xffffu) + rbf(rbf(s0) * g0), __uint_as_float(xw & 0xffff0000u) + rbf(rbf(s1) * g1));
        }
        if (u == 0 && lane == 0 && w == 1) __builtin_amdgcn_raw_buffer_store_b32(T, rws, WS_E, 0, 16);
        STAMPW(14, true);
    }
    STAMPW(14, w == 0);
}

extern "C" void kernel_launch(void* const* d_in, const int* in_sizes, int n_in, void* d_out, int out_size, void* d_ws, size_t ws_size, hipStream_t stream) {
    fused_kernel<<<256, 768, 0, stream>>>((const u16*)d_in[0], (const u16*)d_in[1], (const u16*)d_in[2], (const u16*)d_in[3], (const u16*)d_in[4],
                                          (const u16*)d_in[6], (const u16*)d_in[7], (const u16*)d_in[8], (const u16*)d_in[9], (u16*)d_out, (unsigned char*)d_ws);
}
```

```cpp
#include <hip/hip_runtime.h>
#include <stdint.h>

typedef unsigned short u16;
typedef unsigned v4u __attribute__((ext_vector_type(4)));
typedef float f32x4 __attribute__((ext_vector_type(4)));
typedef short bf16x8 __attribute__((ext_vector_type(8)));
typedef __bf16 bf16x2 __attribute__((ext_vector_type(2)));
#define LAS __attribute__((address_space(3)))

constexpr int D = 576, HKV = 16, SK = 16384, DH = 128, WIN = 4096, F = 4608;
constexpr int WS_E = 0;
constexpr int WS_QP = 4096;
constexpr int WS_HP = WS_QP + 16 * 2 * 4 * 128 * 16;
constexpr int WS_AP = WS_HP + 72 * 2 * 3 * 32 * 16;
constexpr int AP_REC = 136 * 16;
constexpr int WS_SP = WS_AP + 256 * AP_REC;
constexpr int WS_MP = WS_SP + 9 * 2 * 16 * 32 * 16;
constexpr int WS_END = WS_MP + 9 * 2 * 12 * 32 * 16;
#define PFMODE 2
#define PFWAVE 0
#define PFSTRIDE 128
#define PFN ((24576 / PFSTRIDE) / 64)
constexpr unsigned SPIN_CAP = 1u << 18;
constexpr int OOB = 0x7ffffff0;

#define RSRC(p, n) __builtin_amdgcn_make_buffer_rsrc((void*)(p), 0, (n), 0x00020000)
#define LDNT(rs, off) __builtin_amdgcn_raw_buffer_load_b128(rs, off, 0, 2)
#define LDSC(rs, off) __builtin_amdgcn_raw_buffer_load_b128(rs, off, 0, 16)
#define STSC(v, rs, off) __builtin_amdgcn_raw_buffer_store_b128(v, rs, off, 0, 16)

__device__ __forceinline__ float bf2f(unsigned b) { return __uint_as_float(b << 16); }
typedef float f32x2 __attribute__((ext_vector_type(2)));
__device__ __forceinline__ unsigned cvtpk(float lo, float hi) { const f32x2 v = {lo, hi}; return __builtin_bit_cast(unsigned, __builtin_convertvector(v, bf16x2)); }
__device__ __forceinline__ float rbf(float f) { return __uint_as_float(cvtpk(f, f) << 16); }
template <int CTRL> __device__ __forceinline__ float dpp(float x) {
    return __builtin_bit_cast(float, __builtin_amdgcn_mov_dpp(__builtin_bit_cast(int, x), CTRL, 0xf, 0xf, true));
}
__device__ __forceinline__ f32x4 mfma(v4u a, v4u b, f32x4 c) {
    return __builtin_amdgcn_mfma_f32_16x16x32_bf16(__builtin_bit_cast(bf16x8, a), __builtin_bit_cast(bf16x8, b), c, 0, 0, 0);
}
__device__ __forceinline__ float xrow_sum(float x) {
    auto s = __builtin_amdgcn_permlane16_swap(__float_as_uint(x), __float_as_uint(x), false, false);
    x = __uint_as_float(s[0]) + __uint_as_float(s[1]);
    auto t = __builtin_amdgcn_permlane32_swap(__float_as_uint(x), __float_as_uint(x), false, false);
    return __uint_as_float(t[0]) + __uint_as_float(t[1]);
}
__device__ __forceinline__ float xrow_max(float x) {
    auto s = __builtin_amdgcn_permlane16_swap(__float_as_uint(x), __float_as_uint(x), false, false);
    x = fmaxf(__uint_as_float(s[0]), __uint_as_float(s[1]));
    auto t = __builtin_amdgcn_permlane32_swap(__float_as_uint(x), __float_as_uint(x), false, false);
    return fmaxf(__uint_as_float(t[0]), __uint_as_float(t[1]));
}
__device__ __forceinline__ float wave_sum(float v) {
    v += dpp<0xB1>(v); v += dpp<0x4E>(v); v += dpp<0x141>(v); v += dpp<0x140>(v);
    return xrow_sum(v);
}
__device__ __forceinline__ bool tag_ok(v4u p, unsigned T) { return (p.y == T) & (p.w == ~T); }
__device__ __forceinline__ v4u mkpair(float a, float b, unsigned T) { return v4u{__float_as_uint(a), T, __float_as_uint(b), ~T}; }

#define SPIN_PAUSE(n) do { __builtin_amdgcn_s_sleep(n); asm volatile("" :: "s"(ws) : "memory"); } while (0)

constexpr int L_XS = 0;
constexpr int L_Q = 4608;
constexpr int L_ACT = 13312 + 8192;
constexpr int L_ML = 6656;
constexpr int L_FLAG = 7040;
constexpr int L_SCR = 7168;
constexpr int L_RED2 = 9216;
constexpr int L_RED = 13312;
constexpr int L_K = L_RED + 16384;
constexpr int L_BYTES = L_K + 131072;

__global__ void __launch_bounds__(768, 3)
fused_kernel(const u16* __restrict__ x, const u16* __restrict__ kc, const u16* __restrict__ vc, const u16* __restrict__ wq, const u16* __restrict__ wo,
             const u16* __restrict__ w1, const u16* __restrict__ b1, const u16* __restrict__ w2, const u16* __restrict__ b2,
             u16* __restrict__ out, unsigned char* ws) {
    __shared__ __attribute__((aligned(16))) unsigned char lds[L_BYTES];
    const int tid = threadIdx.x, lane = tid & 63, w = __builtin_amdgcn_readfirstlane(tid >> 6), bid = blockIdx.x;
    unsigned pf[PFN];
    if (w == PFWAVE) {
        unsigned long long pc0; unsigned len;
        asm volatile("s_getpc_b64 %0\n2:\n\ts_mov_b32 %1, 1f-2b" : "=s"(pc0), "=s"(len));
        const __amdgpu_buffer_rsrc_t rc = RSRC(pc0 & ~255ull, len + ((unsigned)pc0 & 255u));
#pragma unroll
        for (int i = 0; i < PFN; ++i) pf[i] = __builtin_amdgcn_raw_buffer_load_b32(rc, (i * 64 + lane) * PFSTRIDE, 0, 0);
    }
    __builtin_amdgcn_sched_barrier(0);
    const int s = bid & 7, b = (bid >> 3) & 1, kvh = bid >> 4;
    const int kq = (bid & 15) >> 2, cq = bid & 3;
    const bool has_w1 = bid < 216;
    const int c1 = bid % 72, k1 = bid / 72;
    const bool role_wo = bid < 144, role_w2 = bid >= 144 && bid < 252;
    const int u2 = bid - 144;
    const int tcg = role_wo ? bid % 9 : u2 % 9;
    const int trg = role_wo ? bid / 9 : u2 / 9;
    const int trows = role_wo ? 256 : 384;
    const __amdgpu_buffer_rsrc_t rws = RSRC(ws, WS_END);
    volatile LAS unsigned* qflag = (volatile LAS unsigned*)(LAS unsigned char*)(lds + L_FLAG);
    if (tid < 8) qflag[tid] = 0u;

    const int lrow = lane >> 3, lcol = (lane & 7) * 16;
    const unsigned hm = (lane & 1) ? 0xffff0000u : 0x0000ffffu;
    const int dsel = (lane >> 1) & 3;
    const unsigned M0 = dsel == 0 ? hm : 0u, M1 = dsel == 1 ? hm : 0u, M2 = dsel == 2 ? hm : 0u, M3 = dsel == 3 ? hm : 0u;
#define BDIAG(dup) v4u{(dup) & M0, (dup) & M1, (dup) & M2, (dup) & M3}
    unsigned T;

    v4u xr[3], Wf[24];
    if (w == 8 || w == 9) {
        __builtin_amdgcn_s_setprio(3);
        const __amdgpu_buffer_rsrc_t rx = RSRC(x, 2 * D * 2);
        const __amdgpu_buffer_rsrc_t rw = w == 8 ? RSRC(wq + (size_t)(144 * kq) * 4096 + 256 * kvh + 64 * cq, 144 * 4096 * 2 - 256 * kvh * 2 - 128 * cq)
                                                 : RSRC(w1 + (size_t)(192 * k1) * F + 64 * c1, has_w1 ? 192 * F * 2 - 128 * c1 : 0);
        const int ld = w == 8 ? 4096 * 2 : F * 2;
        const int nld = w == 8 ? 18 : 24;
#pragma unroll
        for (int j = 0; j < 3; ++j) xr[j] = __builtin_amdgcn_raw_buffer_load_b128(rx, (lane + 64 * j) * 16, 0, 0);
#pragma unroll
        for (int j = 0; j < 24; ++j) Wf[j] = LDNT(rw, j < nld ? (8 * j + lrow) * ld + lcol : OOB);
    }
    unsigned ep = 0;
    if (w == 8 || w == 9) ep = __builtin_amdgcn_raw_buffer_load_b32(rws, WS_E, 0, 16);
    __builtin_amdgcn_sched_barrier(0);
    __syncthreads();
    __builtin_amdgcn_sched_barrier(0);

    if (__builtin_expect(w < 8, 1)) {
        const __amdgpu_buffer_rsrc_t rk = RSRC(kc + ((size_t)(b * HKV + kvh) * SK + (SK - WIN) + s * 512 + w * 64) * DH, 64 * 256);
        const __amdgpu_buffer_rsrc_t rv = RSRC(vc + ((size_t)(b * HKV + kvh) * SK + (SK - WIN) + s * 512 + w * 64) * DH, 64 * 256);
        v4u K[16], V[16];
        __builtin_amdgcn_s_setprio(1);
#pragma unroll
        for (int j = 0; j < 16; ++j) K[j] = LDNT(rk, j * 1024 + lane * 16);
        __builtin_amdgcn_sched_barrier(0);
        __builtin_amdgcn_s_setprio(0);
        __builtin_amdgcn_sched_barrier(0);
#pragma unroll
        for (int j = 0; j < 16; ++j) V[j] = LDNT(rv, j * 1024 + lane * 16);
        __builtin_amdgcn_sched_barrier(0);
        if (lane == 0) __hip_atomic_fetch_add((LAS unsigned*)(qflag + 5), 1u, __ATOMIC_RELAXED, __HIP_MEMORY_SCOPE_WORKGROUP);
        const int g = lane >> 4, c = lane & 15;
#pragma unroll
        for (int j = 0; j < 16; ++j) { const int i = 4 * (j & 3) + g; *(v4u*)(lds + L_K + w * 16384 + (j >> 2) * 4096 + i * 256 + ((c ^ (4 * g + (j & 3))) * 16)) = K[j]; }
        if (w == PFWAVE) {
#pragma unroll
            for (int i = 0; i < PFN; ++i) asm volatile("" :: "v"(pf[i]));
        }
        { unsigned spin = 0; while (*qflag == 0u && ++spin < (SPIN_CAP << 4)) __builtin_amdgcn_s_sleep(1); T = qflag[1]; }
        asm volatile("s_waitcnt lgkmcnt(0)" ::: "memory");
        v4u qf[4];
#pragma unroll
        for (int ks = 0; ks < 4; ++ks) qf[ks] = *(const v4u*)(lds + L_Q + (c >> 3) * 256 + (4 * ks + g) * 16);
        f32x4 sc[4];
#pragma unroll
        for (int t = 0; t < 4; ++t) {
            sc[t] = f32x4{0, 0, 0, 0};
#pragma unroll
            for (int ks = 0; ks < 4; ++ks) {
                const v4u ka = *(const v4u*)(lds + L_K + w * 16384 + t * 4096 + (4 * (c & 3) + (c >> 2)) * 256 + (((4 * ks + g) ^ c) * 16));
                sc[t] = mfma(ka, qf[ks], sc[t]);
            }
        }
        float m = -INFINITY;
#pragma unroll
        for (int t = 0; t < 4; ++t)
#pragma unroll
            for (int r = 0; r < 4; ++r) { sc[t][r] = rbf(rbf(sc[t][r]) * 0.088388347648318440f); m = fmaxf(m, sc[t][r]); }
        m = xrow_max(m);
        float l = 0.f;
        constexpr float LOG2E = 1.4426950408889634f;
        const float mm = m * LOG2E;
        unsigned pd[16];
#pragma unroll
        for (int t = 0; t < 4; ++t)
#pragma unroll
            for (int r = 0; r < 4; ++r) { const float p = __builtin_amdgcn_exp2f(sc[t][r] * LOG2E - mm); l += p; pd[4 * t + r] = cvtpk(p, p); }
        l = xrow_sum(l);
        f32x4 acc0 = {0, 0, 0, 0}, acc1 = {0, 0, 0, 0};
#pragma unroll
        for (int t = 0; t < 16; t += 2) { acc0 = mfma(V[t], BDIAG(pd[t]), acc0); acc1 = mfma(V[t + 1], BDIAG(pd[t + 1]), acc1); }
        acc0 += acc1;
        __builtin_amdgcn_s_setprio(3);
        const int hh = (lane >> 3) & 1;
        *(f32x4*)(lds + L_RED + (w * 64 + lane) * 16) = acc0;
        if ((lane & 0x37) == 0) *(float2*)(lds + L_ML + (w * 2 + hh) * 8) = make_float2(mm, l);
        asm volatile("s_waitcnt lgkmcnt(0)" ::: "memory");
        unsigned arr = 0;
        if (lane == 0) arr = __hip_atomic_fetch_add((LAS unsigned*)(qflag + 2), 1u, __ATOMIC_RELAXED, __HIP_MEMORY_SCOPE_WORKGROUP);
        arr = __builtin_amdgcn_readfirstlane(arr);
        asm volatile("" ::: "memory");
        if (arr == 7u) {
            float2 ml[8]; f32x4 pa[8];
#pragma unroll
            for (int i = 0; i < 8; ++i) { ml[i] = *(const float2*)(lds + L_ML + (i * 2 + hh) * 8); pa[i] = *(const f32x4*)(lds + L_RED + (i * 64 + lane) * 16); }
            float M = ml[0].x;
#pragma unroll
            for (int i = 1; i < 8; ++i) M = fmaxf(M, ml[i].x);
            f32x4 a = {0, 0, 0, 0}; float L = 0.f;
#pragma unroll
            for (int i = 0; i < 8; ++i) { const float wt = __builtin_amdgcn_exp2f(ml[i].x - M); a += wt * pa[i]; L += wt * ml[i].y; }
            const int off = WS_AP + bid * AP_REC + lane * 16;
            STSC(mkpair(a[0], a[1], T), rws, off);
            STSC(mkpair(a[2], a[3], T), rws, off + 1024);
            STSC(mkpair(M, L, T), rws, (lane & 0x37) == 0 ? WS_AP + bid * AP_REC + (128 + hh) * 16 : OOB);
            if (lane == 0) qflag[6] = 1u;
        }
        if (role_wo && w < 4) {
            { unsigned spin = 0; while (qflag[6] == 0u && ++spin < (SPIN_CAP << 4)) __builtin_amdgcn_s_sleep(2); }
            const int bb = (w >> 1) & 1, h2 = w & 1;
            const unsigned bit = 1u << (w & 3);
            const int base = WS_AP + (trg * 16 + bb * 8) * AP_REC;
            v4u g[8], gm[8];
            bool ok = false;
            unsigned spin = 0;
            do {
#pragma unroll
                for (int i = 0; i < 8; ++i) { g[i] = LDSC(rws, base + i * AP_REC + ((lane >> 5) * 64 + ((lane >> 3) & 3) * 16 + h2 * 8 + (lane & 7)) * 16); gm[i] = LDSC(rws, base + i * AP_REC + (128 + h2) * 16); }
                bool o = true;
#pragma unroll
                for (int i = 0; i < 8; ++i) o &= tag_ok(g[i], T) & tag_ok(gm[i], T);
                ok = __all(o);
                if (ok) break;
                SPIN_PAUSE(2);
            } while (++spin < SPIN_CAP);
            if (ok) {
                float M = __uint_as_float(gm[0].x);
#pragma unroll
                for (int i = 1; i < 8; ++i) M = fmaxf(M, __uint_as_float(gm[i].x));
                float a0 = 0.f, a1 = 0.f, L = 0.f;
#pragma unroll
                for (int i = 0; i < 8; ++i) {
                    const float wt = __builtin_amdgcn_exp2f(__uint_as_float(gm[i].x) - M);
                    a0 += wt * __uint_as_float(g[i].x); a1 += wt * __uint_as_float(g[i].z); L += wt * __uint_as_float(gm[i].z);
                }
                const float ri = 1.f / L;
                const int dl = h2 * 128 + 32 * ((lane >> 3) & 3) + 16 * (lane >> 5) + (lane & 7);
                unsigned* act = (unsigned*)(lds + L_ACT);
                act[dl * 2 + bb] = cvtpk(a0 * ri, a0 * ri); act[(dl + 8) * 2 + bb] = cvtpk(a1 * ri, a1 * ri);
                asm volatile("s_waitcnt lgkmcnt(0)" ::: "memory");
                if (lane == 0) __hip_atomic_fetch_or((LAS unsigned*)(qflag + 3), bit, __ATOMIC_RELAXED, __HIP_MEMORY_SCOPE_WORKGROUP);
            } else if (spin >= SPIN_CAP) {
                if (lane == 0) __hip_atomic_fetch_or((LAS unsigned*)(qflag + 3), bit, __ATOMIC_RELAXED, __HIP_MEMORY_SCOPE_WORKGROUP);
            }
        }
    } else {
        const int sw = w - 8;
        const __amdgpu_buffer_rsrc_t rt = RSRC(role_wo ? wo : w2, role_wo ? 4096 * D * 2 : (role_w2 ? F * D * 2 : 0));
        if (sw < 2) {
            const int row0 = sw == 0 ? 144 * kq : 192 * k1;
            unsigned char* xsb = lds + L_XS + sw * 2304;
            {
                float xv[3][8];
#pragma unroll
                for (int j = 0; j < 3; ++j)
#pragma unroll
                    for (int k = 0; k < 4; ++k) { xv[j][2 * k] = bf2f(xr[j][k] & 0xffffu); xv[j][2 * k + 1] = __uint_as_float(xr[j][k] & 0xffff0000u); }
                float sj[3];
#pragma unroll
                for (int j = 0; j < 3; ++j) { float t = 0.f;
#pragma unroll
                    for (int k = 0; k < 8; ++k) t += xv[j][k];
                    sj[j] = t; }
                const bool c1b0 = lane < 8, c2ok = lane < 16;
                const float mean0 = wave_sum(sj[0] + (c1b0 ? sj[1] : 0.f)) * (1.f / D);
                const float mean1 = wave_sum((c1b0 ? 0.f : sj[1]) + sj[2]) * (1.f / D);
                const float mj[3] = {mean0, c1b0 ? mean0 : mean1, mean1};
#pragma unroll
                for (int j = 0; j < 3; ++j) { float t = 0.f;
#pragma unroll
                    for (int k = 0; k < 8; ++k) { const float d = xv[j][k] - mj[j]; t += d * d; }
                    sj[j] = t; }
                if (!c2ok) sj[2] = 0.f;
                const float var0 = wave_sum(sj[0] + (c1b0 ? sj[1] : 0.f)) * (1.f / D);
                const float var1 = wave_sum((c1b0 ? 0.f : sj[1]) + sj[2]) * (1.f / D);
                const float mb0 = rbf(mean0), mb1 = rbf(mean1);
                const float den0 = rbf(sqrtf(rbf(rbf(var0) + 1e-5f))), den1 = rbf(sqrtf(rbf(rbf(var1) + 1e-5f)));
                const float ri0 = 1.f / den0, ri1 = 1.f / den1;
                const float mbj[3] = {mb0, c1b0 ? mb0 : mb1, mb1}, dnj[3] = {ri0, c1b0 ? ri0 : ri1, ri1};
#pragma unroll
                for (int j = 0; j < 3; ++j) {
                    v4u o;
#pragma unroll
                    for (int k = 0; k < 4; ++k) o[k] = cvtpk(rbf(xv[j][2 * k] - mbj[j]) * dnj[j], rbf(xv[j][2 * k + 1] - mbj[j]) * dnj[j]);
                    if (j < 2 || c2ok) *(v4u*)(xsb + (lane + 64 * j) * 16) = o;
                }
            }
            __builtin_amdgcn_fence(__ATOMIC_ACQ_REL, "wavefront");
            asm volatile("" ::: "memory");
            const u16* xs = (const u16*)xsb;
            f32x4 af[2] = {{0, 0, 0, 0}, {0, 0, 0, 0}}, ag[2] = {{0, 0, 0, 0}, {0, 0, 0, 0}};
#pragma unroll
            for (int j = 0; j < 18; ++j) {
                const int r = min(row0 + 8 * j + lrow, D - 1);
#pragma unroll
                for (int bb = 0; bb < 2; ++bb) { const unsigned v = xs[bb * D + r]; const unsigned dup = v | (v << 16);
                    if (j & 1) ag[bb] = mfma(Wf[j], BDIAG(dup), ag[bb]); else af[bb] = mfma(Wf[j], BDIAG(dup), af[bb]); }
            }
            if (sw == 1) {
#pragma unroll
                for (int j = 18; j < 24; ++j) {
                    const int r = min(row0 + 8 * j + lrow, D - 1);
#pragma unroll
                    for (int bb = 0; bb < 2; ++bb) { const unsigned v = xs[bb * D + r]; const unsigned dup = v | (v << 16);
                        if (j & 1) ag[bb] = mfma(Wf[j], BDIAG(dup), ag[bb]); else af[bb] = mfma(Wf[j], BDIAG(dup), af[bb]); }
                }
            }
            af[0] += ag[0]; af[1] += ag[1];
            T = __builtin_amdgcn_readfirstlane(ep) + 1u;
            float* scr = (float*)(lds + L_SCR + sw * 1024);
            if ((lane >> 5) == ((lane >> 3) & 1)) {
                const int r1 = lane >> 5, cb = 32 * ((lane >> 4) & 1) + (lane & 7);
#pragma unroll
                for (int bb = 0; bb < 2; ++bb)
#pragma unroll
                    for (int r = 0; r < 4; ++r) scr[(r1 * 2 + bb) * 64 + cb + 8 * r] = af[bb][r];
            }
            __builtin_amdgcn_fence(__ATOMIC_ACQ_REL, "wavefront");
            asm volatile("" ::: "memory");
            {
                const int bb = lane >> 5, pr = lane & 31;
                const float2 t0 = *(const float2*)&scr[bb * 64 + 2 * pr], t1 = *(const float2*)&scr[(2 + bb) * 64 + 2 * pr];
                const int off = sw == 0 ? WS_QP + (((kvh * 2 + bb) * 4 + kq) * 128 + cq * 32 + pr) * 16
                                        : (has_w1 ? WS_HP + (((c1 * 2 + bb) * 3 + k1) * 32 + pr) * 16 : OOB);
                STSC(mkpair(t0.x + t1.x, t0.y + t1.y, T), rws, off);
            }
            if (sw == 0) {
                const int qoff = WS_QP + ((kvh * 2 + b) * 4 * 128 + lane) * 16;
                v4u gq[8]; bool ok; unsigned spin = 0;
                do {
#pragma unroll
                    for (int i = 0; i < 8; ++i) gq[i] = LDSC(rws, qoff + (i >> 1) * 128 * 16 + (i & 1) * 64 * 16);
                    ok = true;
#pragma unroll
                    for (int i = 0; i < 8; ++i) ok &= tag_ok(gq[i], T);
                    if (__all(ok)) break;
                    SPIN_PAUSE(1);
                } while (++spin < SPIN_CAP);
#pragma unroll
                for (int hf2 = 0; hf2 < 2; ++hf2) {
                    const float q0 = (__uint_as_float(gq[hf2].x) + __uint_as_float(gq[2 + hf2].x)) + (__uint_as_float(gq[4 + hf2].x) + __uint_as_float(gq[6 + hf2].x));
                    const float q1 = (__uint_as_float(gq[hf2].z) + __uint_as_float(gq[2 + hf2].z)) + (__uint_as_float(gq[4 + hf2].z) + __uint_as_float(gq[6 + hf2].z));
                    ((unsigned*)(lds + L_Q))[hf2 * 64 + lane] = cvtpk(q0, q1);
                }
                if (lane == 0) qflag[1] = T;
                asm volatile("s_waitcnt lgkmcnt(0)" ::: "memory");
                if (lane == 0) *qflag = 1u;
            }
            __builtin_amdgcn_s_setprio(0);
        }
        __builtin_amdgcn_sched_barrier(0);
        { unsigned spin = 0; while (qflag[5] < 8u && ++spin < (SPIN_CAP << 4)) __builtin_amdgcn_s_sleep(2); }
        v4u Wt[12];
#pragma unroll
        for (int j = 0; j < 12; ++j) { const int u = sw + 4 * j; Wt[j] = LDNT(rt, (8 * u < trows) ? ((trows * trg + 8 * u + lrow) * D + 64 * tcg) * 2 + lcol : OOB); }
        __builtin_amdgcn_sched_barrier(0);
        if (sw >= 2 || (sw == 1 && !has_w1)) {
            unsigned spin = 0; while (*qflag == 0u && ++spin < (SPIN_CAP << 4)) __builtin_amdgcn_s_sleep(4);
            T = qflag[1];
        }
        if (role_w2) {
            const int st = tid - 512;
            const __amdgpu_buffer_rsrc_t rb1 = RSRC(b1, F * 2);
            v4u gh[2][3]; unsigned b1v[2]; int hoff[2];
#pragma unroll
            for (int e = 0; e < 2; ++e) {
                const int it = st + 256 * e; const bool valid = it < 384;
                const int hbb = it / 192, hpr = it % 192, hf = 384 * trg + 2 * hpr;
                hoff[e] = valid ? WS_HP + ((((hf >> 6) * 2 + hbb) * 3) * 32 + ((hf & 63) >> 1)) * 16 : OOB;
                b1v[e] = __builtin_amdgcn_raw_buffer_load_b32(rb1, valid ? hf * 2 : OOB, 0, 0);
            }
            bool ok; unsigned spin = 0;
            do {
                ok = true;
#pragma unroll
                for (int e = 0; e < 2; ++e)
#pragma unroll
                    for (int i = 0; i < 3; ++i) { gh[e][i] = LDSC(rws, hoff[e] == OOB ? OOB : hoff[e] + i * 32 * 16); }
#pragma unroll
                for (int e = 0; e < 2; ++e)
#pragma unroll
                    for (int i = 0; i < 3; ++i) ok &= (hoff[e] == OOB) | tag_ok(gh[e][i], T);
                if (__all(ok)) break;
                SPIN_PAUSE(2);
            } while (++spin < SPIN_CAP);
#pragma unroll
            for (int e = 0; e < 2; ++e) {
                const int it = st + 256 * e;
                float h0 = __uint_as_float(gh[e][0].x) + __uint_as_float(gh[e][1].x) + __uint_as_float(gh[e][2].x);
                float h1 = __uint_as_float(gh[e][0].z) + __uint_as_float(gh[e][1].z) + __uint_as_float(gh[e][2].z);
                h0 = rbf(rbf(h0) + bf2f(b1v[e] & 0xffffu)); h1 = rbf(rbf(h1) + __uint_as_float(b1v[e] & 0xffff0000u));
                h0 = h0 > 0.f ? h0 : 0.f; h1 = h1 > 0.f ? h1 : 0.f;
                h0 *= h0; h1 *= h1;
                const int hbb = it / 192, hpr = it % 192;
                if (it < 384) { unsigned* act = (unsigned*)(lds + L_ACT); act[(2 * hpr) * 2 + hbb] = cvtpk(h0, h0); act[(2 * hpr + 1) * 2 + hbb] = cvtpk(h1, h1); }
            }
            asm volatile("s_waitcnt lgkmcnt(0)" ::: "memory");
            if (lane == 0) __hip_atomic_fetch_or((LAS unsigned*)(qflag + 3), 1u << sw, __ATOMIC_RELAXED, __HIP_MEMORY_SCOPE_WORKGROUP);
        }
        if (role_wo || role_w2) {
            { unsigned spin = 0; while (qflag[3] != 15u && ++spin < (SPIN_CAP << 4)) __builtin_amdgcn_s_sleep(0); }
            asm volatile("" ::: "memory");
            const unsigned* act = (const unsigned*)(lds + L_ACT);
            f32x4 at[2][2] = {{{0, 0, 0, 0}, {0, 0, 0, 0}}, {{0, 0, 0, 0}, {0, 0, 0, 0}}};
            typedef unsigned v2u __attribute__((ext_vector_type(2)));
            v2u ad[8];
#pragma unroll
            for (int j = 0; j < 8; ++j) ad[j] = *(const v2u*)&act[(8 * (sw + 4 * j) + lrow) * 2];
#pragma unroll
            for (int j = 0; j < 8; ++j) { at[0][j & 1] = mfma(Wt[j], BDIAG(ad[j].x), at[0][j & 1]); at[1][j & 1] = mfma(Wt[j], BDIAG(ad[j].y), at[1][j & 1]); }
            if (role_w2) {
#pragma unroll
                for (int j = 8; j < 12; ++j) { const v2u a2 = *(const v2u*)&act[(8 * (sw + 4 * j) + lrow) * 2]; at[0][j & 1] = mfma(Wt[j], BDIAG(a2.x), at[0][j & 1]); at[1][j & 1] = mfma(Wt[j], BDIAG(a2.y), at[1][j & 1]); }
            }
            at[0][0] += at[0][1]; at[1][0] += at[1][1];
            float* red2 = (float*)(lds + L_RED2);
            if ((lane >> 5) == ((lane >> 3) & 1)) {
                const int r1 = lane >> 5, cb = 32 * ((lane >> 4) & 1) + (lane & 7);
#pragma unroll
                for (int bb = 0; bb < 2; ++bb)
#pragma unroll
                    for (int r = 0; r < 4; ++r) red2[((sw * 2 + r1) * 2 + bb) * 64 + cb + 8 * r] = at[bb][0][r];
            }
            asm volatile("s_waitcnt lgkmcnt(0)" ::: "memory");
            unsigned arr = 0;
            if (lane == 0) arr = __hip_atomic_fetch_add((LAS unsigned*)(qflag + 4), 1u, __ATOMIC_RELAXED, __HIP_MEMORY_SCOPE_WORKGROUP);
            arr = __builtin_amdgcn_readfirstlane(arr);
            asm volatile("" ::: "memory");
            if (arr == 3u) {
                const int bb = lane >> 5, pr = lane & 31;
                float v0 = 0.f, v1 = 0.f;
#pragma unroll
                for (int i = 0; i < 8; ++i) { const float2 t = *(const float2*)&red2[(i * 2 + bb) * 64 + 2 * pr]; v0 += t.x; v1 += t.y; }
                const int off = role_wo ? WS_SP + (((tcg * 2 + bb) * 16 + trg) * 32 + pr) * 16 : WS_MP + (((tcg * 2 + bb) * 12 + trg) * 32 + pr) * 16;
                STSC(mkpair(v0, v1, T), rws, off);
            }
        }
        asm volatile("1:");
    }

    if (bid >= 144 && bid < 216 && w == 1) {
        const int u = bid - 144, cg = u >> 3, fb = (u >> 2) & 1, q4 = u & 3;
        const int p8 = lane >> 3, j = lane & 7;
        const int col = 64 * cg + 16 * q4 + 2 * j;
        const unsigned xw = *(const unsigned*)(x + fb * D + col), bw = *(const unsigned*)(b2 + col);
        const int offs = WS_SP + (((cg * 2 + fb) * 16 + p8) * 32 + 8 * q4 + j) * 16;
        const int offm = WS_MP + (((cg * 2 + fb) * 12 + p8) * 32 + 8 * q4 + j) * 16;
        v4u g[4]; bool ok; unsigned spin = 0;
        do {
            g[0] = LDSC(rws, offs); g[1] = LDSC(rws, offs + 8 * 32 * 16); g[2] = LDSC(rws, offm); g[3] = LDSC(rws, p8 < 4 ? offm + 8 * 32 * 16 : OOB);
            ok = tag_ok(g[0], T) & tag_ok(g[1], T) & tag_ok(g[2], T) & ((p8 >= 4) | tag_ok(g[3], T));
            if (__all(ok)) break;
            SPIN_PAUSE(1);
        } while (++spin < SPIN_CAP);
        float s0 = __uint_as_float(g[0].x) + __uint_as_float(g[1].x), s1 = __uint_as_float(g[0].z) + __uint_as_float(g[1].z);
        float m0 = __uint_as_float(g[2].x) + __uint_as_float(g[3].x), m1 = __uint_as_float(g[2].z) + __uint_as_float(g[3].z);
        s0 += dpp<0x128>(s0); s1 += dpp<0x128>(s1); m0 += dpp<0x128>(m0); m1 += dpp<0x128>(m1);
        s0 = xrow_sum(s0); s1 = xrow_sum(s1); m0 = xrow_sum(m0); m1 = xrow_sum(m1);
        if (lane < 8) {
            const float mv0 = rbf(rbf(m0) + bf2f(bw & 0xffffu)), mv1 = rbf(rbf(m1) + __uint_as_float(bw & 0xffff0000u));
            const float g0 = rbf(mv0 / (1.f + __expf(-mv0))), g1 = rbf(mv1 / (1.f + __expf(-mv1)));
            *(unsigned*)(out + fb * D + col) = cvtpk(bf2f(xw & 0xffffu) + rbf(rbf(s0) * g0), __uint_as_float(xw & 0xffff0000u) + rbf(rbf(s1) * g1));
        }
        if (u == 0 && lane == 0) __builtin_amdgcn_raw_buffer_store_b32(T, rws, WS_E, 0, 16);
    }
}

extern "C" void kernel_launch(void* const* d_in, const int* in_sizes, int n_in, void* d_out, int out_size, void* d_ws, size_t ws_size, hipStream_t stream) {
    fused_kernel<<<256, 768, 0, stream>>>((const u16*)d_in[0], (const u16*)d_in[1], (const u16*)d_in[2], (const u16*)d_in[3], (const u16*)d_in[4],
                                          (const u16*)d_in[6], (const u16*)d_in[7], (const u16*)d_in[8], (const u16*)d_in[9], (u16*)d_out, (unsigned char*)d_ws);
}
```

```cpp
#include <hip/hip_runtime.h>
#include <stdint.h>

typedef unsigned short u16;
typedef unsigned v4u __attribute__((ext_vector_type(4)));
typedef float f32x4 __attribute__((ext_vector_type(4)));
typedef short bf16x8 __attribute__((ext_vector_type(8)));
typedef __bf16 bf16x2 __attribute__((ext_vector_type(2)));
#define LAS __attribute__((address_space(3)))

constexpr int D = 576, HKV = 16, SK = 16384, DH = 128, WIN = 4096, F = 4608;
constexpr int WS_E = 0;
constexpr int WS_QP = 4096;
constexpr int WS_HP = WS_QP + 16 * 2 * 4 * 128 * 16;
constexpr int WS_AP = WS_HP + 72 * 2 * 3 * 32 * 16;
constexpr int AP_REC = 136 * 16;
constexpr int WS_SP = WS_AP + 256 * AP_REC;
constexpr int WS_MP = WS_SP + 9 * 2 * 16 * 32 * 16;
constexpr int WS_END = WS_MP + 9 * 2 * 12 * 32 * 16;
#define FRONTPOS 0
#define EARLYB 0
#define PFMODE 2
#define PFWAVE 10
#define PFSTRIDE 64
#define PFN ((24576 / PFSTRIDE) / 64)
constexpr unsigned SPIN_CAP = 1u << 18;
constexpr int OOB = 0x7ffffff0;

#define RSRC(p, n) __builtin_amdgcn_make_buffer_rsrc((void*)(p), 0, (n), 0x00020000)
#define LDNT(rs, off) __builtin_amdgcn_raw_buffer_load_b128(rs, off, 0, 2)
#define LDSC(rs, off) __builtin_amdgcn_raw_buffer_load_b128(rs, off, 0, 16)
#define STSC(v, rs, off) __builtin_amdgcn_raw_buffer_store_b128(v, rs, off, 0, 16)

__device__ __forceinline__ float bf2f(unsigned b) { return __uint_as_float(b << 16); }
typedef float f32x2 __attribute__((ext_vector_type(2)));
__device__ __forceinline__ unsigned cvtpk(float lo, float hi) { const f32x2 v = {lo, hi}; return __builtin_bit_cast(unsigned, __builtin_convertvector(v, bf16x2)); }
__device__ __forceinline__ float rbf(float f) { return __uint_as_float(cvtpk(f, f) << 16); }
template <int CTRL> __device__ __forceinline__ float dpp(float x) {
    return __builtin_bit_cast(float, __builtin_amdgcn_mov_dpp(__builtin_bit_cast(int, x), CTRL, 0xf, 0xf, true));
}
__device__ __forceinline__ f32x4 mfma(v4u a, v4u b, f32x4 c) {
    return __builtin_amdgcn_mfma_f32_16x16x32_bf16(__builtin_bit_cast(bf16x8, a), __builtin_bit_cast(bf16x8, b), c, 0, 0, 0);
}
__device__ __forceinline__ float xrow_sum(float x) {
    auto s = __builtin_amdgcn_permlane16_swap(__float_as_uint(x), __float_as_uint(x), false, false);
    x = __uint_as_float(s[0]) + __uint_as_float(s[1]);
    auto t = __builtin_amdgcn_permlane32_swap(__float_as_uint(x), __float_as_uint(x), false, false);
    return __uint_as_float(t[0]) + __uint_as_float(t[1]);
}
__device__ __forceinline__ float xrow_max(float x) {
    auto s = __builtin_amdgcn_permlane16_swap(__float_as_uint(x), __float_as_uint(x), false, false);
    x = fmaxf(__uint_as_float(s[0]), __uint_as_float(s[1]));
    auto t = __builtin_amdgcn_permlane32_swap(__float_as_uint(x), __float_as_uint(x), false, false);
    return fmaxf(__uint_as_float(t[0]), __uint_as_float(t[1]));
}
__device__ __forceinline__ float wave_sum(float v) {
    v += dpp<0xB1>(v); v += dpp<0x4E>(v); v += dpp<0x141>(v); v += dpp<0x140>(v);
    return xrow_sum(v);
}
__device__ __forceinline__ bool tag_ok(v4u p, unsigned T) { return (p.y == T) & (p.w == ~T); }
__device__ __forceinline__ v4u mkpair(float a, float b, unsigned T) { return v4u{__float_as_uint(a), T, __float_as_uint(b), ~T}; }

#define SPIN_PAUSE(n) do { __builtin_amdgcn_s_sleep(n); asm volatile("" :: "s"(ws) : "memory"); } while (0)

constexpr int L_XS = 0;
constexpr int L_Q = 4608;
constexpr int L_ACT = 13312 + 8192;
constexpr int L_ML = 6656;
constexpr int L_FLAG = 7040;
constexpr int L_SCR = 7168;
constexpr int L_RED2 = 9216;
constexpr int L_RED = 13312;
constexpr int L_K = L_RED + 16384;
constexpr int L_BYTES = L_K + 131072;

__global__ void __launch_bounds__(768, 3)
fused_kernel(const u16* __restrict__ x, const u16* __restrict__ kc, const u16* __restrict__ vc, const u16* __restrict__ wq, const u16* __restrict__ wo,
             const u16* __restrict__ w1, const u16* __restrict__ b1, const u16* __restrict__ w2, const u16* __restrict__ b2,
             u16* __restrict__ out, unsigned char* ws) {
    __shared__ __attribute__((aligned(16))) unsigned char lds[L_BYTES];
    const int tid = threadIdx.x, lane = tid & 63, w = __builtin_amdgcn_readfirstlane(tid >> 6), bid = blockIdx.x;
#define KARGS asm volatile("" :: "s"(x), "s"(kc), "s"(vc), "s"(wq), "s"(wo), "s"(w1), "s"(b1), "s"(w2), "s"(b2), "s"(out), "s"(ws))
    unsigned pf[PFN];
    if (w == PFWAVE) {
        unsigned long long pc0; unsigned len;
        asm volatile("s_getpc_b64 %0\n2:\n\ts_mov_b32 %1, 1f-2b" : "=s"(pc0), "=s"(len));
        const __amdgpu_buffer_rsrc_t rc = RSRC(pc0 & ~255ull, len + ((unsigned)pc0 & 255u));
#pragma unroll
        for (int i = 0; i < PFN; ++i) pf[i] = __builtin_amdgcn_raw_buffer_load_b32(rc, (i * 64 + lane) * PFSTRIDE, 0, 0);
    }
    __builtin_amdgcn_sched_barrier(0);
    volatile LAS unsigned* qflag = (volatile LAS unsigned*)(LAS unsigned char*)(lds + L_FLAG);
    if (tid < 8) qflag[tid] = 0u;
    const int s = bid & 7, b = (bid >> 3) & 1, kvh = bid >> 4;
    const int lrow = lane >> 3, lcol = (lane & 7) * 16;
    const __amdgpu_buffer_rsrc_t rws = RSRC(ws, WS_END);
    int ka0 = lane * 16, ka1 = ka0 + 4096, ka2 = ka0 + 8192, ka3 = ka0 + 12288;
    const unsigned kvo = (unsigned)(((b * HKV + kvh) * SK + (SK - WIN) + s * 512 + (w & 7) * 64) * (DH * 2));
    asm volatile("" : "+v"(ka0), "+v"(ka1), "+v"(ka2), "+v"(ka3));
    KARGS;
#define FRONT_LOADS \
    if (w == 8 || w == 9) { \
        __builtin_amdgcn_s_setprio(3); \
        const __amdgpu_buffer_rsrc_t rx = RSRC(x, 2 * D * 2); \
        const int e_k1 = bid / 72, e_c1 = bid - 72 * e_k1; \
        const bool q8 = w == 8; \
        const int ld = q8 ? 4096 * 2 : F * 2; \
        const int colb = q8 ? 512 * kvh + 128 * (bid & 3) : 128 * e_c1; \
        const int rowb = q8 ? 144 * ((bid & 15) >> 2) : 192 * e_k1; \
        const int nrec = q8 ? 144 * 4096 * 2 : (bid < 216 ? 192 * F * 2 : 0); \
        const __amdgpu_buffer_rsrc_t rw = RSRC((const char*)(q8 ? wq : w1) + (unsigned)(rowb * ld), nrec); \
_Pragma("unroll") \
        for (int j = 0; j < 3; ++j) xr[j] = __builtin_amdgcn_raw_buffer_load_b128(rx, ka0 + 1024 * j, 0, 0); \
        int off = lrow * ld + lcol + colb; const int step = 8 * ld; \
_Pragma("unroll") \
        for (int j = 0; j < 24; ++j) { Wf[j] = LDNT(rw, off); off += step; asm volatile("" : "+v"(off)); } \
        ep = __builtin_amdgcn_raw_buffer_load_b32(rws, WS_E, 0, 16); \
    }
    v4u xr[3], Wf[24];
    unsigned ep = 0;
    FRONT_LOADS
    __builtin_amdgcn_sched_barrier(0);
    __syncthreads();
    __builtin_amdgcn_sched_barrier(0);
#define ROLES \
    int bi2 = bid; asm volatile("" : "+s"(bi2)); \
    int ln2 = lane; asm volatile("" : "+v"(ln2)); \
    const bool role_wo = bi2 < 144, role_w2 = bi2 >= 144 && bi2 < 252; \
    const int u2 = role_wo ? bi2 : bi2 - 144; \
    const int trg = u2 / 9, tcg = u2 - 9 * trg;            \
    const unsigned hm = (ln2 & 1) ? 0xffff0000u : 0x0000ffffu;        \
    const int dsel = (ln2 >> 1) & 3; \
    const unsigned M0 = dsel == 0 ? hm : 0u, M1 = dsel == 1 ? hm : 0u, M2 = dsel == 2 ? hm : 0u, M3 = dsel == 3 ? hm : 0u;
#define BDIAG(dup) v4u{(dup) & M0, (dup) & M1, (dup) & M2, (dup) & M3}
    unsigned T;

    if (__builtin_expect(w < 8, 1)) {
        v4u K[16], V[16];
        {
            const __amdgpu_buffer_rsrc_t rk = RSRC((const char*)kc + kvo, 64 * 256);
            const __amdgpu_buffer_rsrc_t rv = RSRC((const char*)vc + kvo, 64 * 256);
            __builtin_amdgcn_s_setprio(1);
#pragma unroll
            for (int j = 0; j < 16; ++j) K[j] = LDNT(rk, (j < 4 ? ka0 : j < 8 ? ka1 : j < 12 ? ka2 : ka3) + (j & 3) * 1024);
            __builtin_amdgcn_sched_barrier(0);
            __builtin_amdgcn_s_setprio(0);
            __builtin_amdgcn_sched_barrier(0);
#pragma unroll
            for (int j = 0; j < 16; ++j) V[j] = LDNT(rv, (j < 4 ? ka0 : j < 8 ? ka1 : j < 12 ? ka2 : ka3) + (j & 3) * 1024);
        }
        __builtin_amdgcn_sched_barrier(0);
        ROLES
        if (lane == 0) __hip_atomic_fetch_add((LAS unsigned*)(qflag + 5), 1u, __ATOMIC_RELAXED, __HIP_MEMORY_SCOPE_WORKGROUP);
        const int g = lane >> 4, c = lane & 15;
#pragma unroll
        for (int j = 0; j < 16; ++j) { const int i = 4 * (j & 3) + g; *(v4u*)(lds + L_K + w * 16384 + (j >> 2) * 4096 + i * 256 + ((c ^ (4 * g + (j & 3))) * 16)) = K[j]; }
        { unsigned spin = 0; while (*qflag == 0u && ++spin < (SPIN_CAP << 4)) __builtin_amdgcn_s_sleep(1); T = qflag[1]; }
        asm volatile("s_waitcnt lgkmcnt(0)" ::: "memory");
        v4u qf[4];
#pragma unroll
        for (int ks = 0; ks < 4; ++ks) qf[ks] = *(const v4u*)(lds + L_Q + (c >> 3) * 256 + (4 * ks + g) * 16);
        f32x4 sc[4];
#pragma unroll
        for (int t = 0; t < 4; ++t) {
            sc[t] = f32x4{0, 0, 0, 0};
#pragma unroll
            for (int ks = 0; ks < 4; ++ks) {
                const v4u ka = *(const v4u*)(lds + L_K + w * 16384 + t * 4096 + (4 * (c & 3) + (c >> 2)) * 256 + (((4 * ks + g) ^ c) * 16));
                sc[t] = mfma(ka, qf[ks], sc[t]);
            }
        }
        float m = -INFINITY;
#pragma unroll
        for (int t = 0; t < 4; ++t)
#pragma unroll
            for (int r = 0; r < 4; ++r) { sc[t][r] = rbf(rbf(sc[t][r]) * 0.088388347648318440f); m = fmaxf(m, sc[t][r]); }
        m = xrow_max(m);
        float l = 0.f;
        constexpr float LOG2E = 1.4426950408889634f;
        const float mm = m * LOG2E;
        unsigned pd[16];
#pragma unroll
        for (int t = 0; t < 4; ++t)
#pragma unroll
            for (int r = 0; r < 4; ++r) { const float p = __builtin_amdgcn_exp2f(sc[t][r] * LOG2E - mm); l += p; pd[4 * t + r] = cvtpk(p, p); }
        l = xrow_sum(l);
        f32x4 acc0 = {0, 0, 0, 0}, acc1 = {0, 0, 0, 0};
#pragma unroll
        for (int t = 0; t < 16; t += 2) { acc0 = mfma(V[t], BDIAG(pd[t]), acc0); acc1 = mfma(V[t + 1], BDIAG(pd[t + 1]), acc1); }
        acc0 += acc1;
        __builtin_amdgcn_s_setprio(3);
        const int hh = (lane >> 3) & 1;
        *(f32x4*)(lds + L_RED + (w * 64 + lane) * 16) = acc0;
        if ((lane & 0x37) == 0) *(float2*)(lds + L_ML + (w * 2 + hh) * 8) = make_float2(mm, l);
        asm volatile("s_waitcnt lgkmcnt(0)" ::: "memory");
        unsigned arr = 0;
        if (lane == 0) arr = __hip_atomic_fetch_add((LAS unsigned*)(qflag + 2), 1u, __ATOMIC_RELAXED, __HIP_MEMORY_SCOPE_WORKGROUP);
        arr = __builtin_amdgcn_readfirstlane(arr);
        asm volatile("" ::: "memory");
        if (arr == 7u) {
            float2 ml[8]; f32x4 pa[8];
#pragma unroll
            for (int i = 0; i < 8; ++i) { ml[i] = *(const float2*)(lds + L_ML + (i * 2 + hh) * 8); pa[i] = *(const f32x4*)(lds + L_RED + (i * 64 + lane) * 16); }
            float M = ml[0].x;
#pragma unroll
            for (int i = 1; i < 8; ++i) M = fmaxf(M, ml[i].x);
            f32x4 a = {0, 0, 0, 0}; float L = 0.f;
#pragma unroll
            for (int i = 0; i < 8; ++i) { const float wt = __builtin_amdgcn_exp2f(ml[i].x - M); a += wt * pa[i]; L += wt * ml[i].y; }
            const int off = WS_AP + bid * AP_REC + lane * 16;
            STSC(mkpair(a[0], a[1], T), rws, off);
            STSC(mkpair(a[2], a[3], T), rws, off + 1024);
            STSC(mkpair(M, L, T), rws, (lane & 0x37) == 0 ? WS_AP + bid * AP_REC + (128 + hh) * 16 : OOB);
            if (lane == 0) qflag[6] = 1u;
        }
        if (role_wo && w < 4) {
            { unsigned spin = 0; while (qflag[6] == 0u && ++spin < (SPIN_CAP << 4)) __builtin_amdgcn_s_sleep(2); }
            const int bb = (w >> 1) & 1, h2 = w & 1;
            const unsigned bit = 1u << (w & 3);
            const int base = WS_AP + (trg * 16 + bb * 8) * AP_REC;
            v4u g[8], gm[8];
            bool ok = false;
            unsigned spin = 0;
            do {
#pragma unroll
                for (int i = 0; i < 8; ++i) { g[i] = LDSC(rws, base + i * AP_REC + ((lane >> 5) * 64 + ((lane >> 3) & 3) * 16 + h2 * 8 + (lane & 7)) * 16); gm[i] = LDSC(rws, base + i * AP_REC + (128 + h2) * 16); }
                bool o = true;
#pragma unroll
                for (int i = 0; i < 8; ++i) o &= tag_ok(g[i], T) & tag_ok(gm[i], T);
                ok = __all(o);
                if (ok) break;
                SPIN_PAUSE(2);
            } while (++spin < SPIN_CAP);
            if (ok) {
                float M = __uint_as_float(gm[0].x);
#pragma unroll
                for (int i = 1; i < 8; ++i) M = fmaxf(M, __uint_as_float(gm[i].x));
                float a0 = 0.f, a1 = 0.f, L = 0.f;
#pragma unroll
                for (int i = 0; i < 8; ++i) {
                    const float wt = __builtin_amdgcn_exp2f(__uint_as_float(gm[i].x) - M);
                    a0 += wt * __uint_as_float(g[i].x); a1 += wt * __uint_as_float(g[i].z); L += wt * __uint_as_float(gm[i].z);
                }
                const float ri = 1.f / L;
                const int dl = h2 * 128 + 32 * ((lane >> 3) & 3) + 16 * (lane >> 5) + (lane & 7);
                unsigned* act = (unsigned*)(lds + L_ACT);
                act[dl * 2 + bb] = cvtpk(a0 * ri, a0 * ri); act[(dl + 8) * 2 + bb] = cvtpk(a1 * ri, a1 * ri);
                asm volatile("s_waitcnt lgkmcnt(0)" ::: "memory");
                if (lane == 0) __hip_atomic_fetch_or((LAS unsigned*)(qflag + 3), bit, __ATOMIC_RELAXED, __HIP_MEMORY_SCOPE_WORKGROUP);
            } else if (spin >= SPIN_CAP) {
                if (lane == 0) __hip_atomic_fetch_or((LAS unsigned*)(qflag + 3), bit, __ATOMIC_RELAXED, __HIP_MEMORY_SCOPE_WORKGROUP);
            }
        }
    } else {
        const int sw = w - 8;
        ROLES
        const int kq = (bi2 & 15) >> 2, cq = bi2 & 3;
        const bool has_w1 = bi2 < 216;
        const int k1 = bi2 / 72, c1 = bi2 - 72 * k1;
        const int trows = role_wo ? 256 : 384;
        if (w == PFWAVE) {
#pragma unroll
            for (int i = 0; i < PFN; ++i) asm volatile("" :: "v"(pf[i]));
        }
        const __amdgpu_buffer_rsrc_t rt = RSRC(role_wo ? wo : w2, role_wo ? 4096 * D * 2 : (role_w2 ? F * D * 2 : 0));
        if (sw < 2) {
            const int row0 = sw == 0 ? 144 * kq : 192 * k1;
            unsigned char* xsb = lds + L_XS + sw * 2304;
            {
                float xv[3][8];
#pragma unroll
                for (int j = 0; j < 3; ++j)
#pragma unroll
                    for (int k = 0; k < 4; ++k) { xv[j][2 * k] = bf2f(xr[j][k] & 0xffffu); xv[j][2 * k + 1] = __uint_as_float(xr[j][k] & 0xffff0000u); }
                float sj[3];
#pragma unroll
                for (int j = 0; j < 3; ++j) { float t = 0.f;
#pragma unroll
                    for (int k = 0; k < 8; ++k) t += xv[j][k];
                    sj[j] = t; }
                const bool c1b0 = lane < 8, c2ok = lane < 16;
                const float mean0 = wave_sum(sj[0] + (c1b0 ? sj[1] : 0.f)) * (1.f / D);
                const float mean1 = wave_sum((c1b0 ? 0.f : sj[1]) + sj[2]) * (1.f / D);
                const float mj[3] = {mean0, c1b0 ? mean0 : mean1, mean1};
#pragma unroll
                for (int j = 0; j < 3; ++j) { float t = 0.f;
#pragma unroll
                    for (int k = 0; k < 8; ++k) { const float d = xv[j][k] - mj[j]; t += d * d; }
                    sj[j] = t; }
                if (!c2ok) sj[2] = 0.f;
                const float var0 = wave_sum(sj[0] + (c1b0 ? sj[1] : 0.f)) * (1.f / D);
                const float var1 = wave_sum((c1b0 ? 0.f : sj[1]) + sj[2]) * (1.f / D);
                const float mb0 = rbf(mean0), mb1 = rbf(mean1);
                const float den0 = rbf(sqrtf(rbf(rbf(var0) + 1e-5f))), den1 = rbf(sqrtf(rbf(rbf(var1) + 1e-5f)));
                const float ri0 = 1.f / den0, ri1 = 1.f / den1;
                const float mbj[3] = {mb0, c1b0 ? mb0 : mb1, mb1}, dnj[3] = {ri0, c1b0 ? ri0 : ri1, ri1};
#pragma unroll
                for (int j = 0; j < 3; ++j) {
                    v4u o;
#pragma unroll
                    for (int k = 0; k < 4; ++k) o[k] = cvtpk(rbf(xv[j][2 * k] - mbj[j]) * dnj[j], rbf(xv[j][2 * k + 1] - mbj[j]) * dnj[j]);
                    if (j < 2 || c2ok) *(v4u*)(xsb + (lane + 64 * j) * 16) = o;
                }
            }
            __builtin_amdgcn_fence(__ATOMIC_ACQ_REL, "wavefront");
            asm volatile("" ::: "memory");
            const u16* xs = (const u16*)xsb;
            f32x4 af[2] = {{0, 0, 0, 0}, {0, 0, 0, 0}}, ag[2] = {{0, 0, 0, 0}, {0, 0, 0, 0}};
#pragma unroll
            for (int j = 0; j < 18; ++j) {
                const int r = min(row0 + 8 * j + lrow, D - 1);
#pragma unroll
                for (int bb = 0; bb < 2; ++bb) { const unsigned v = xs[bb * D + r]; const unsigned dup = v | (v << 16);
                    if (j & 1) ag[bb] = mfma(Wf[j], BDIAG(dup), ag[bb]); else af[bb] = mfma(Wf[j], BDIAG(dup), af[bb]); }
            }
            if (sw == 1) {
#pragma unroll
                for (int j = 18; j < 24; ++j) {
                    const int r = min(row0 + 8 * j + lrow, D - 1);
#pragma unroll
                    for (int bb = 0; bb < 2; ++bb) { const unsigned v = xs[bb * D + r]; const unsigned dup = v | (v << 16);
                        if (j & 1) ag[bb] = mfma(Wf[j], BDIAG(dup), ag[bb]); else af[bb] = mfma(Wf[j], BDIAG(dup), af[bb]); }
                }
            }
            af[0] += ag[0]; af[1] += ag[1];
            T = __builtin_amdgcn_readfirstlane(ep) + 1u;
            float* scr = (float*)(lds + L_SCR + sw * 1024);
            if ((lane >> 5) == ((lane >> 3) & 1)) {
                const int r1 = lane >> 5, cb = 32 * ((lane >> 4) & 1) + (lane & 7);
#pragma unroll
                for (int bb = 0; bb < 2; ++bb)
#pragma unroll
                    for (int r = 0; r < 4; ++r) scr[(r1 * 2 + bb) * 64 + cb + 8 * r] = af[bb][r];
            }
            __builtin_amdgcn_fence(__ATOMIC_ACQ_REL, "wavefront");
            asm volatile("" ::: "memory");
            {
                const int bb = lane >> 5, pr = lane & 31;
                const float2 t0 = *(const float2*)&scr[bb * 64 + 2 * pr], t1 = *(const float2*)&scr[(2 + bb) * 64 + 2 * pr];
                const int off = sw == 0 ? WS_QP + (((kvh * 2 + bb) * 4 + kq) * 128 + cq * 32 + pr) * 16
                                        : (has_w1 ? WS_HP + (((c1 * 2 + bb) * 3 + k1) * 32 + pr) * 16 : OOB);
                STSC(mkpair(t0.x + t1.x, t0.y + t1.y, T), rws, off);
            }
            if (sw == 0) {
                const int qoff = WS_QP + ((kvh * 2 + b) * 4 * 128 + lane) * 16;
                v4u gq[8]; bool ok; unsigned spin = 0;
                do {
#pragma unroll
                    for (int i = 0; i < 8; ++i) gq[i] = LDSC(rws, qoff + (i >> 1) * 128 * 16 + (i & 1) * 64 * 16);
                    ok = true;
#pragma unroll
                    for (int i = 0; i < 8; ++i) ok &= tag_ok(gq[i], T);
                    if (__all(ok)) break;
                    SPIN_PAUSE(1);
                } while (++spin < SPIN_CAP);
#pragma unroll
                for (int hf2 = 0; hf2 < 2; ++hf2) {
                    const float q0 = (__uint_as_float(gq[hf2].x) + __uint_as_float(gq[2 + hf2].x)) + (__uint_as_float(gq[4 + hf2].x) + __uint_as_float(gq[6 + hf2].x));
                    const float q1 = (__uint_as_float(gq[hf2].z) + __uint_as_float(gq[2 + hf2].z)) + (__uint_as_float(gq[4 + hf2].z) + __uint_as_float(gq[6 + hf2].z));
                    ((unsigned*)(lds + L_Q))[hf2 * 64 + lane] = cvtpk(q0, q1);
                }
                if (lane == 0) qflag[1] = T;
                asm volatile("s_waitcnt lgkmcnt(0)" ::: "memory");
                if (lane == 0) *qflag = 1u;
            }
            __builtin_amdgcn_s_setprio(0);
        }
        __builtin_amdgcn_sched_barrier(0);
        { unsigned spin = 0; while (qflag[5] < 8u && ++spin < (SPIN_CAP << 4)) __builtin_amdgcn_s_sleep(2); }
        v4u Wt[12];
#pragma unroll
        for (int j = 0; j < 12; ++j) { const int u = sw + 4 * j; Wt[j] = LDNT(rt, (8 * u < trows) ? ((trows * trg + 8 * u + lrow) * D + 64 * tcg) * 2 + lcol : OOB); }
        __builtin_amdgcn_sched_barrier(0);
        if (sw >= 2 || (sw == 1 && !has_w1)) {
            unsigned spin = 0; while (*qflag == 0u && ++spin < (SPIN_CAP << 4)) __builtin_amdgcn_s_sleep(4);
            T = qflag[1];
        }
        if (role_w2) {
            const int st = tid - 512;
            const __amdgpu_buffer_rsrc_t rb1 = RSRC(b1, F * 2);
            v4u gh[2][3]; unsigned b1v[2]; int hoff[2];
#pragma unroll
            for (int e = 0; e < 2; ++e) {
                const int it = st + 256 * e; const bool valid = it < 384;
                const int hbb = it / 192, hpr = it % 192, hf = 384 * trg + 2 * hpr;
                hoff[e] = valid ? WS_HP + ((((hf >> 6) * 2 + hbb) * 3) * 32 + ((hf & 63) >> 1)) * 16 : OOB;
                b1v[e] = __builtin_amdgcn_raw_buffer_load_b32(rb1, valid ? hf * 2 : OOB, 0, 0);
            }
            bool ok; unsigned spin = 0;
            do {
                ok = true;
#pragma unroll
                for (int e = 0; e < 2; ++e)
#pragma unroll
                    for (int i = 0; i < 3; ++i) { gh[e][i] = LDSC(rws, hoff[e] == OOB ? OOB : hoff[e] + i * 32 * 16); }
#pragma unroll
                for (int e = 0; e < 2; ++e)
#pragma unroll
                    for (int i = 0; i < 3; ++i) ok &= (hoff[e] == OOB) | tag_ok(gh[e][i], T);
                if (__all(ok)) break;
                SPIN_PAUSE(2);
            } while (++spin < SPIN_CAP);
#pragma unroll
            for (int e = 0; e < 2; ++e) {
                const int it = st + 256 * e;
                float h0 = __uint_as_float(gh[e][0].x) + __uint_as_float(gh[e][1].x) + __uint_as_float(gh[e][2].x);
                float h1 = __uint_as_float(gh[e][0].z) + __uint_as_float(gh[e][1].z) + __uint_as_float(gh[e][2].z);
                h0 = rbf(rbf(h0) + bf2f(b1v[e] & 0xffffu)); h1 = rbf(rbf(h1) + __uint_as_float(b1v[e] & 0xffff0000u));
                h0 = h0 > 0.f ? h0 : 0.f; h1 = h1 > 0.f ? h1 : 0.f;
                h0 *= h0; h1 *= h1;
                const int hbb = it / 192, hpr = it % 192;
                if (it < 384) { unsigned* act = (unsigned*)(lds + L_ACT); act[(2 * hpr) * 2 + hbb] = cvtpk(h0, h0); act[(2 * hpr + 1) * 2 + hbb] = cvtpk(h1, h1); }
            }
            asm volatile("s_waitcnt lgkmcnt(0)" ::: "memory");
            if (lane == 0) __hip_atomic_fetch_or((LAS unsigned*)(qflag + 3), 1u << sw, __ATOMIC_RELAXED, __HIP_MEMORY_SCOPE_WORKGROUP);
        }
        if (role_wo || role_w2) {
            { unsigned spin = 0; while (qflag[3] != 15u && ++spin < (SPIN_CAP << 4)) __builtin_amdgcn_s_sleep(0); }
            asm volatile("" ::: "memory");
            const unsigned* act = (const unsigned*)(lds + L_ACT);
            f32x4 at[2][2] = {{{0, 0, 0, 0}, {0, 0, 0, 0}}, {{0, 0, 0, 0}, {0, 0, 0, 0}}};
            typedef unsigned v2u __attribute__((ext_vector_type(2)));
            v2u ad[8];
#pragma unroll
            for (int j = 0; j < 8; ++j) ad[j] = *(const v2u*)&act[(8 * (sw + 4 * j) + lrow) * 2];
#pragma unroll
            for (int j = 0; j < 8; ++j) { at[0][j & 1] = mfma(Wt[j], BDIAG(ad[j].x), at[0][j & 1]); at[1][j & 1] = mfma(Wt[j], BDIAG(ad[j].y), at[1][j & 1]); }
            if (role_w2) {
#pragma unroll
                for (int j = 8; j < 12; ++j) { const v2u a2 = *(const v2u*)&act[(8 * (sw + 4 * j) + lrow) * 2]; at[0][j & 1] = mfma(Wt[j], BDIAG(a2.x), at[0][j & 1]); at[1][j & 1] = mfma(Wt[j], BDIAG(a2.y), at[1][j & 1]); }
            }
            at[0][0] += at[0][1]; at[1][0] += at[1][1];
            float* red2 = (float*)(lds + L_RED2);
            if ((lane >> 5) == ((lane >> 3) & 1)) {
                const int r1 = lane >> 5, cb = 32 * ((lane >> 4) & 1) + (lane & 7);
#pragma unroll
                for (int bb = 0; bb < 2; ++bb)
#pragma unroll
                    for (int r = 0; r < 4; ++r) red2[((sw * 2 + r1) * 2 + bb) * 64 + cb + 8 * r] = at[bb][0][r];
            }
            asm volatile("s_waitcnt lgkmcnt(0)" ::: "memory");
            unsigned arr = 0;
            if (lane == 0) arr = __hip_atomic_fetch_add((LAS unsigned*)(qflag + 4), 1u, __ATOMIC_RELAXED, __HIP_MEMORY_SCOPE_WORKGROUP);
            arr = __builtin_amdgcn_readfirstlane(arr);
            asm volatile("" ::: "memory");
            if (arr == 3u) {
                const int bb = lane >> 5, pr = lane & 31;
                float v0 = 0.f, v1 = 0.f;
#pragma unroll
                for (int i = 0; i < 8; ++i) { const float2 t = *(const float2*)&red2[(i * 2 + bb) * 64 + 2 * pr]; v0 += t.x; v1 += t.y; }
                const int off = role_wo ? WS_SP + (((tcg * 2 + bb) * 16 + trg) * 32 + pr) * 16 : WS_MP + (((tcg * 2 + bb) * 12 + trg) * 32 + pr) * 16;
                STSC(mkpair(v0, v1, T), rws, off);
            }
        }
        asm volatile("1:");
    }

    if (bid >= 144 && bid < 216 && w == 1) {
        const int u = bid - 144, cg = u >> 3, fb = (u >> 2) & 1, q4 = u & 3;
        const int p8 = lane >> 3, j = lane & 7;
        const int col = 64 * cg + 16 * q4 + 2 * j;
        const unsigned xw = *(const unsigned*)(x + fb * D + col), bw = *(const unsigned*)(b2 + col);
        const int offs = WS_SP + (((cg * 2 + fb) * 16 + p8) * 32 + 8 * q4 + j) * 16;
        const int offm = WS_MP + (((cg * 2 + fb) * 12 + p8) * 32 + 8 * q4 + j) * 16;
        v4u g[4]; bool ok; unsigned spin = 0;
        do {
            g[0] = LDSC(rws, offs); g[1] = LDSC(rws, offs + 8 * 32 * 16); g[2] = LDSC(rws, offm); g[3] = LDSC(rws, p8 < 4 ? offm + 8 * 32 * 16 : OOB);
            ok = tag_ok(g[0], T) & tag_ok(g[1], T) & tag_ok(g[2], T) & ((p8 >= 4) | tag_ok(g[3], T));
            if (__all(ok)) break;
            SPIN_PAUSE(1);
        } while (++spin < SPIN_CAP);
        float s0 = __uint_as_float(g[0].x) + __uint_as_float(g[1].x), s1 = __uint_as_float(g[0].z) + __uint_as_float(g[1].z);
        float m0 = __uint_as_float(g[2].x) + __uint_as_float(g[3].x), m1 = __uint_as_float(g[2].z) + __uint_as_float(g[3].z);
        s0 += dpp<0x128>(s0); s1 += dpp<0x128>(s1); m0 += dpp<0x128>(m0); m1 += dpp<0x128>(m1);
        s0 = xrow_sum(s0); s1 = xrow_sum(s1); m0 = xrow_sum(m0); m1 = xrow_sum(m1);
        if (lane < 8) {
            const float mv0 = rbf(rbf(m0) + bf2f(bw & 0xffffu)), mv1 = rbf(rbf(m1) + __uint_as_float(bw & 0xffff0000u));
            const float g0 = rbf(mv0 / (1.f + __expf(-mv0))), g1 = rbf(mv1 / (1.f + __expf(-mv1)));
            *(unsigned*)(out + fb * D + col) = cvtpk(bf2f(xw & 0xffffu) + rbf(rbf(s0) * g0), __uint_as_float(xw & 0xffff0000u) + rbf(rbf(s1) * g1));
        }
        if (u == 0 && lane == 0) __builtin_amdgcn_raw_buffer_store_b32(T, rws, WS_E, 0, 16);
    }
}

extern "C" void kernel_launch(void* const* d_in, const int* in_sizes, int n_in, void* d_out, int out_size, void* d_ws, size_t ws_size, hipStream_t stream) {
    fused_kernel<<<256, 768, 0, stream>>>((const u16*)d_in[0], (const u16*)d_in[1], (const u16*)d_in[2], (const u16*)d_in[3], (const u16*)d_in[4],
                                          (const u16*)d_in[6], (const u16*)d_in[7], (const u16*)d_in[8], (const u16*)d_in[9], (u16*)d_out, (unsigned char*)d_ws);
}
```

```cpp
#include <hip/hip_runtime.h>
#include <stdint.h>

typedef unsigned short u16;
typedef unsigned v4u __attribute__((ext_vector_type(4)));
typedef float f32x4 __attribute__((ext_vector_type(4)));
typedef short bf16x8 __attribute__((ext_vector_type(8)));
typedef __bf16 bf16x2 __attribute__((ext_vector_type(2)));
#define LAS __attribute__((address_space(3)))

constexpr int D = 576, HKV = 16, SK = 16384, DH = 128, WIN = 4096, F = 4608;
constexpr int WS_E = 0;
constexpr int WS_QP = 4096;
constexpr int WS_HP = WS_QP + 16 * 2 * 4 * 128 * 16;
constexpr int WS_AP = WS_HP + 72 * 2 * 3 * 32 * 16;
constexpr int AP_REC = 136 * 16;
constexpr int WS_SP = WS_AP + 256 * AP_REC;
constexpr int WS_MP = WS_SP + 9 * 2 * 16 * 32 * 16;
constexpr int WS_END = WS_MP + 9 * 2 * 12 * 32 * 16;
#define NOUNROLL _Pragma("clang loop unroll(disable)")
constexpr unsigned SPIN_CAP = 1u << 18;
constexpr int OOB = 0x7ffffff0;

#define RSRC(p, n) __builtin_amdgcn_make_buffer_rsrc((void*)(p), 0, (n), 0x00020000)
#define LDNT(rs, off) __builtin_amdgcn_raw_buffer_load_b128(rs, off, 0, 2)
#define LDSC(rs, off) __builtin_amdgcn_raw_buffer_load_b128(rs, off, 0, 16)
#define STSC(v, rs, off) __builtin_amdgcn_raw_buffer_store_b128(v, rs, off, 0, 16)

__device__ __forceinline__ float bf2f(unsigned b) { return __uint_as_float(b << 16); }
typedef float f32x2 __attribute__((ext_vector_type(2)));
__device__ __forceinline__ unsigned cvtpk(float lo, float hi) { const f32x2 v = {lo, hi}; return __builtin_bit_cast(unsigned, __builtin_convertvector(v, bf16x2)); }
__device__ __forceinline__ float rbf(float f) { return __uint_as_float(cvtpk(f, f) << 16); }
template <int CTRL> __device__ __forceinline__ float dpp(float x) {
    return __builtin_bit_cast(float, __builtin_amdgcn_mov_dpp(__builtin_bit_cast(int, x), CTRL, 0xf, 0xf, true));
}
__device__ __forceinline__ f32x4 mfma(v4u a, v4u b, f32x4 c) {
    return __builtin_amdgcn_mfma_f32_16x16x32_bf16(__builtin_bit_cast(bf16x8, a), __builtin_bit_cast(bf16x8, b), c, 0, 0, 0);
}
__device__ __forceinline__ float xrow_sum(float x) {
    auto s = __builtin_amdgcn_permlane16_swap(__float_as_uint(x), __float_as_uint(x), false, false);
    x = __uint_as_float(s[0]) + __uint_as_float(s[1]);
    auto t = __builtin_amdgcn_permlane32_swap(__float_as_uint(x), __float_as_uint(x), false, false);
    return __uint_as_float(t[0]) + __uint_as_float(t[1]);
}
__device__ __forceinline__ float xrow_max(float x) {
    auto s = __builtin_amdgcn_permlane16_swap(__float_as_uint(x), __float_as_uint(x), false, false);
    x = fmaxf(__uint_as_float(s[0]), __uint_as_float(s[1]));
    auto t = __builtin_amdgcn_permlane32_swap(__float_as_uint(x), __float_as_uint(x), false, false);
    return fmaxf(__uint_as_float(t[0]), __uint_as_float(t[1]));
}
__device__ __forceinline__ float wave_sum(float v) {
    v += dpp<0xB1>(v); v += dpp<0x4E>(v); v += dpp<0x141>(v); v += dpp<0x140>(v);
    return xrow_sum(v);
}
__device__ __forceinline__ bool tag_ok(v4u p, unsigned T) { return (p.y == T) & (p.w == ~T); }
__device__ __forceinline__ v4u mkpair(float a, float b, unsigned T) { return v4u{__float_as_uint(a), T, __float_as_uint(b), ~T}; }

#define SPIN_PAUSE(n) do { __builtin_amdgcn_s_sleep(n); asm volatile("" :: "s"(ws) : "memory"); } while (0)

constexpr int L_XS = 0;
constexpr int L_Q = 4608;
constexpr int L_ACT = 13312 + 8192;
constexpr int L_ML = 6656;
constexpr int L_FLAG = 7040;
constexpr int L_SCR = 7168;
constexpr int L_RED2 = 9216;
constexpr int L_RED = 13312;
constexpr int L_K = L_RED + 16384;
constexpr int L_BYTES = L_K + 131072;

__global__ void __launch_bounds__(768, 3)
fused_kernel(const u16* __restrict__ x, const u16* __restrict__ kc, const u16* __restrict__ vc, const u16* __restrict__ wq, const u16* __restrict__ wo,
             const u16* __restrict__ w1, const u16* __restrict__ b1, const u16* __restrict__ w2, const u16* __restrict__ b2,
             u16* __restrict__ out, unsigned char* ws) {
    __shared__ __attribute__((aligned(16))) unsigned char lds[L_BYTES];
    const int tid = threadIdx.x, lane = tid & 63, w = __builtin_amdgcn_readfirstlane(tid >> 6), bid = blockIdx.x;
#define KARGS asm volatile("" :: "s"(x), "s"(kc), "s"(vc), "s"(wq), "s"(wo), "s"(w1), "s"(b1), "s"(w2), "s"(b2), "s"(out), "s"(ws))
    unsigned pf3;
    {
        unsigned long long pc0; unsigned len;
        asm volatile("s_getpc_b64 %0\n2:\n\ts_mov_b32 %1, 1f-2b" : "=s"(pc0), "=s"(len));
        const __amdgpu_buffer_rsrc_t rc = RSRC(pc0 & ~255ull, len + ((unsigned)pc0 & 255u));
        pf3 = __builtin_amdgcn_raw_buffer_load_b32(rc, tid * 32, 0, 0);
    }
    __builtin_amdgcn_sched_barrier(0);
    volatile LAS unsigned* qflag = (volatile LAS unsigned*)(LAS unsigned char*)(lds + L_FLAG);
    if (tid < 8) qflag[tid] = 0u;
    const int s = bid & 7, b = (bid >> 3) & 1, kvh = bid >> 4;
    const int lrow = lane >> 3, lcol = (lane & 7) * 16;
    const __amdgpu_buffer_rsrc_t rws = RSRC(ws, WS_END);
    int ka0 = lane * 16, ka1 = ka0 + 4096, ka2 = ka0 + 8192, ka3 = ka0 + 12288;
    const unsigned kvo = (unsigned)(((b * HKV + kvh) * SK + (SK - WIN) + s * 512 + (w & 7) * 64) * (DH * 2));
    asm volatile("" : "+v"(ka0), "+v"(ka1), "+v"(ka2), "+v"(ka3));
    KARGS;
#define FRONT_LOADS \
    if (w == 8 || w == 9) { \
        __builtin_amdgcn_s_setprio(3); \
        const __amdgpu_buffer_rsrc_t rx = RSRC(x, 2 * D * 2); \
        const int e_k1 = bid / 72, e_c1 = bid - 72 * e_k1; \
        const bool q8 = w == 8; \
        const int ld = q8 ? 4096 * 2 : F * 2; \
        const int colb = q8 ? 512 * kvh + 128 * (bid & 3) : 128 * e_c1; \
        const int rowb = q8 ? 144 * ((bid & 15) >> 2) : 192 * e_k1; \
        const int nrec = q8 ? 144 * 4096 * 2 : (bid < 216 ? 192 * F * 2 : 0); \
        const __amdgpu_buffer_rsrc_t rw = RSRC((const char*)(q8 ? wq : w1) + (unsigned)(rowb * ld), nrec); \
_Pragma("unroll") \
        for (int j = 0; j < 3; ++j) xr[j] = __builtin_amdgcn_raw_buffer_load_b128(rx, ka0 + 1024 * j, 0, 0); \
        int off = lrow * ld + lcol + colb; const int step = 8 * ld; \
_Pragma("unroll") \
        for (int j = 0; j < 24; ++j) { Wf[j] = LDNT(rw, off); off += step; asm volatile("" : "+v"(off)); } \
        ep = __builtin_amdgcn_raw_buffer_load_b32(rws, WS_E, 0, 16); \
    }
    v4u xr[3], Wf[24];
    unsigned ep = 0;
    FRONT_LOADS
    __builtin_amdgcn_sched_barrier(0);
    __syncthreads();
    __builtin_amdgcn_sched_barrier(0);
#define ROLES \
    int bi2 = bid; asm volatile("" : "+s"(bi2)); \
    int ln2 = lane; asm volatile("" : "+v"(ln2)); \
    const bool role_wo = bi2 < 144, role_w2 = bi2 >= 144 && bi2 < 252; \
    const int u2 = role_wo ? bi2 : bi2 - 144; \
    const int trg = u2 / 9, tcg = u2 - 9 * trg;            \
    const unsigned hm = (ln2 & 1) ? 0xffff0000u : 0x0000ffffu;        \
    const int dsel = (ln2 >> 1) & 3; \
    const unsigned M0 = dsel == 0 ? hm : 0u, M1 = dsel == 1 ? hm : 0u, M2 = dsel == 2 ? hm : 0u, M3 = dsel == 3 ? hm : 0u;
#define BDIAG(dup) v4u{(dup) & M0, (dup) & M1, (dup) & M2, (dup) & M3}
    unsigned T;

    if (__builtin_expect(w < 8, 1)) {
        v4u K[16], V[16];
        {
            const __amdgpu_buffer_rsrc_t rk = RSRC((const char*)kc + kvo, 64 * 256);
            const __amdgpu_buffer_rsrc_t rv = RSRC((const char*)vc + kvo, 64 * 256);
            __builtin_amdgcn_s_setprio(1);
#pragma unroll
            for (int j = 0; j < 16; ++j) K[j] = LDNT(rk, (j < 4 ? ka0 : j < 8 ? ka1 : j < 12 ? ka2 : ka3) + (j & 3) * 1024);
            __builtin_amdgcn_sched_barrier(0);
            __builtin_amdgcn_s_setprio(0);
            __builtin_amdgcn_sched_barrier(0);
#pragma unroll
            for (int j = 0; j < 16; ++j) V[j] = LDNT(rv, (j < 4 ? ka0 : j < 8 ? ka1 : j < 12 ? ka2 : ka3) + (j & 3) * 1024);
        }
        __builtin_amdgcn_sched_barrier(0);
        ROLES
        if (lane == 0) __hip_atomic_fetch_add((LAS unsigned*)(qflag + 5), 1u, __ATOMIC_RELAXED, __HIP_MEMORY_SCOPE_WORKGROUP);
        const int g = lane >> 4, c = lane & 15;
#pragma unroll
        for (int j = 0; j < 16; ++j) { const int i = 4 * (j & 3) + g; *(v4u*)(lds + L_K + w * 16384 + (j >> 2) * 4096 + i * 256 + ((c ^ (4 * g + (j & 3))) * 16)) = K[j]; }
        asm volatile("" :: "v"(pf3));
        { unsigned spin = 0; NOUNROLL while (*qflag == 0u && ++spin < (SPIN_CAP << 4)) __builtin_amdgcn_s_sleep(1); T = qflag[1]; }
        asm volatile("s_waitcnt lgkmcnt(0)" ::: "memory");
        v4u qf[4];
#pragma unroll
        for (int ks = 0; ks < 4; ++ks) qf[ks] = *(const v4u*)(lds + L_Q + (c >> 3) * 256 + (4 * ks + g) * 16);
        f32x4 sc[4];
#pragma unroll
        for (int t = 0; t < 4; ++t) {
            sc[t] = f32x4{0, 0, 0, 0};
#pragma unroll
            for (int ks = 0; ks < 4; ++ks) {
                const v4u ka = *(const v4u*)(lds + L_K + w * 16384 + t * 4096 + (4 * (c & 3) + (c >> 2)) * 256 + (((4 * ks + g) ^ c) * 16));
                sc[t] = mfma(ka, qf[ks], sc[t]);
            }
        }
        float m = -INFINITY;
#pragma unroll
        for (int t = 0; t < 4; ++t)
#pragma unroll
            for (int r = 0; r < 4; ++r) { sc[t][r] = rbf(rbf(sc[t][r]) * 0.088388347648318440f); m = fmaxf(m, sc[t][r]); }
        m = xrow_max(m);
        float l = 0.f;
        constexpr float LOG2E = 1.4426950408889634f;
        const float mm = m * LOG2E;
        unsigned pd[16];
#pragma unroll
        for (int t = 0; t < 4; ++t)
#pragma unroll
            for (int r = 0; r < 4; ++r) { const float p = __builtin_amdgcn_exp2f(sc[t][r] * LOG2E - mm); l += p; pd[4 * t + r] = cvtpk(p, p); }
        l = xrow_sum(l);
        f32x4 acc0 = {0, 0, 0, 0}, acc1 = {0, 0, 0, 0};
#pragma unroll
        for (int t = 0; t < 16; t += 2) { acc0 = mfma(V[t], BDIAG(pd[t]), acc0); acc1 = mfma(V[t + 1], BDIAG(pd[t + 1]), acc1); }
        acc0 += acc1;
        __builtin_amdgcn_s_setprio(3);
        const int hh = (lane >> 3) & 1;
        *(f32x4*)(lds + L_RED + (w * 64 + lane) * 16) = acc0;
        if ((lane & 0x37) == 0) *(float2*)(lds + L_ML + (w * 2 + hh) * 8) = make_float2(mm, l);
        asm volatile("s_waitcnt lgkmcnt(0)" ::: "memory");
        unsigned arr = 0;
        if (lane == 0) arr = __hip_atomic_fetch_add((LAS unsigned*)(qflag + 2), 1u, __ATOMIC_RELAXED, __HIP_MEMORY_SCOPE_WORKGROUP);
        arr = __builtin_amdgcn_readfirstlane(arr);
        asm volatile("" ::: "memory");
        if (arr == 7u) {
            float2 ml[8]; f32x4 pa[8];
#pragma unroll
            for (int i = 0; i < 8; ++i) { ml[i] = *(const float2*)(lds + L_ML + (i * 2 + hh) * 8); pa[i] = *(const f32x4*)(lds + L_RED + (i * 64 + lane) * 16); }
            float M = ml[0].x;
#pragma unroll
            for (int i = 1; i < 8; ++i) M = fmaxf(M, ml[i].x);
            f32x4 a = {0, 0, 0, 0}; float L = 0.f;
#pragma unroll
            for (int i = 0; i < 8; ++i) { const float wt = __builtin_amdgcn_exp2f(ml[i].x - M); a += wt * pa[i]; L += wt * ml[i].y; }
            const int off = WS_AP + bid * AP_REC + lane * 16;
            STSC(mkpair(a[0], a[1], T), rws, off);
            STSC(mkpair(a[2], a[3], T), rws, off + 1024);
            STSC(mkpair(M, L, T), rws, (lane & 0x37) == 0 ? WS_AP + bid * AP_REC + (128 + hh) * 16 : OOB);
            if (lane == 0) qflag[6] = 1u;
        }
        if (role_wo && w < 4) {
            { unsigned spin = 0; NOUNROLL while (qflag[6] == 0u && ++spin < (SPIN_CAP << 4)) __builtin_amdgcn_s_sleep(2); }
            const int bb = (w >> 1) & 1, h2 = w & 1;
            const unsigned bit = 1u << (w & 3);
            const int base = WS_AP + (trg * 16 + bb * 8) * AP_REC;
            v4u g[8], gm[8];
            bool ok = false;
            unsigned spin = 0;
            NOUNROLL do {
#pragma unroll
                for (int i = 0; i < 8; ++i) { g[i] = LDSC(rws, base + i * AP_REC + ((lane >> 5) * 64 + ((lane >> 3) & 3) * 16 + h2 * 8 + (lane & 7)) * 16); gm[i] = LDSC(rws, base + i * AP_REC + (128 + h2) * 16); }
                bool o = true;
#pragma unroll
                for (int i = 0; i < 8; ++i) o &= tag_ok(g[i], T) & tag_ok(gm[i], T);
                ok = __all(o);
                if (ok) break;
                SPIN_PAUSE(2);
            } while (++spin < SPIN_CAP);
            if (ok) {
                float M = __uint_as_float(gm[0].x);
#pragma unroll
                for (int i = 1; i < 8; ++i) M = fmaxf(M, __uint_as_float(gm[i].x));
                float a0 = 0.f, a1 = 0.f, L = 0.f;
#pragma unroll
                for (int i = 0; i < 8; ++i) {
                    const float wt = __builtin_amdgcn_exp2f(__uint_as_float(gm[i].x) - M);
                    a0 += wt * __uint_as_float(g[i].x); a1 += wt * __uint_as_float(g[i].z); L += wt * __uint_as_float(gm[i].z);
                }
                const float ri = 1.f / L;
                const int dl = h2 * 128 + 32 * ((lane >> 3) & 3) + 16 * (lane >> 5) + (lane & 7);
                unsigned* act = (unsigned*)(lds + L_ACT);
                act[dl * 2 + bb] = cvtpk(a0 * ri, a0 * ri); act[(dl + 8) * 2 + bb] = cvtpk(a1 * ri, a1 * ri);
                asm volatile("s_waitcnt lgkmcnt(0)" ::: "memory");
                if (lane == 0) __hip_atomic_fetch_or((LAS unsigned*)(qflag + 3), bit, __ATOMIC_RELAXED, __HIP_MEMORY_SCOPE_WORKGROUP);
            } else if (spin >= SPIN_CAP) {
                if (lane == 0) __hip_atomic_fetch_or((LAS unsigned*)(qflag + 3), bit, __ATOMIC_RELAXED, __HIP_MEMORY_SCOPE_WORKGROUP);
            }
        }
    } else {
        const int sw = w - 8;
        ROLES
        const int kq = (bi2 & 15) >> 2, cq = bi2 & 3;
        const bool has_w1 = bi2 < 216;
        const int k1 = bi2 / 72, c1 = bi2 - 72 * k1;
        const int trows = role_wo ? 256 : 384;
        const __amdgpu_buffer_rsrc_t rt = RSRC(role_wo ? wo : w2, role_wo ? 4096 * D * 2 : (role_w2 ? F * D * 2 : 0));
        if (sw < 2) {
            const int row0 = sw == 0 ? 144 * kq : 192 * k1;
            unsigned char* xsb = lds + L_XS + sw * 2304;
            {
                float xv[3][8];
#pragma unroll
                for (int j = 0; j < 3; ++j)
#pragma unroll
                    for (int k = 0; k < 4; ++k) { xv[j][2 * k] = bf2f(xr[j][k] & 0xffffu); xv[j][2 * k + 1] = __uint_as_float(xr[j][k] & 0xffff0000u); }
                float sj[3];
#pragma unroll
                for (int j = 0; j < 3; ++j) { float t = 0.f;
#pragma unroll
                    for (int k = 0; k < 8; ++k) t += xv[j][k];
                    sj[j] = t; }
                const bool c1b0 = lane < 8, c2ok = lane < 16;
                const float mean0 = wave_sum(sj[0] + (c1b0 ? sj[1] : 0.f)) * (1.f / D);
                const float mean1 = wave_sum((c1b0 ? 0.f : sj[1]) + sj[2]) * (1.f / D);
                const float mj[3] = {mean0, c1b0 ? mean0 : mean1, mean1};
#pragma unroll
                for (int j = 0; j < 3; ++j) { float t = 0.f;
#pragma unroll
                    for (int k = 0; k < 8; ++k) { const float d = xv[j][k] - mj[j]; t += d * d; }
                    sj[j] = t; }
                if (!c2ok) sj[2] = 0.f;
                const float var0 = wave_sum(sj[0] + (c1b0 ? sj[1] : 0.f)) * (1.f / D);
                const float var1 = wave_sum((c1b0 ? 0.f : sj[1]) + sj[2]) * (1.f / D);
                const float mb0 = rbf(mean0), mb1 = rbf(mean1);
                const float den0 = rbf(sqrtf(rbf(rbf(var0) + 1e-5f))), den1 = rbf(sqrtf(rbf(rbf(var1) + 1e-5f)));
                const float ri0 = 1.f / den0, ri1 = 1.f / den1;
                const float mbj[3] = {mb0, c1b0 ? mb0 : mb1, mb1}, dnj[3] = {ri0, c1b0 ? ri0 : ri1, ri1};
#pragma unroll
                for (int j = 0; j < 3; ++j) {
                    v4u o;
#pragma unroll
                    for (int k = 0; k < 4; ++k) o[k] = cvtpk(rbf(xv[j][2 * k] - mbj[j]) * dnj[j], rbf(xv[j][2 * k + 1] - mbj[j]) * dnj[j]);
                    if (j < 2 || c2ok) *(v4u*)(xsb + (lane + 64 * j) * 16) = o;
                }
            }
            __builtin_amdgcn_fence(__ATOMIC_ACQ_REL, "wavefront");
            asm volatile("" ::: "memory");
            const u16* xs = (const u16*)xsb;
            f32x4 af[2] = {{0, 0, 0, 0}, {0, 0, 0, 0}}, ag[2] = {{0, 0, 0, 0}, {0, 0, 0, 0}};
#pragma unroll
            for (int j = 0; j < 18; ++j) {
                const int r = min(row0 + 8 * j + lrow, D - 1);
#pragma unroll
                for (int bb = 0; bb < 2; ++bb) { const unsigned v = xs[bb * D + r]; const unsigned dup = v | (v << 16);
                    if (j & 1) ag[bb] = mfma(Wf[j], BDIAG(dup), ag[bb]); else af[bb] = mfma(Wf[j], BDIAG(dup), af[bb]); }
            }
            if (sw == 1) {
#pragma unroll
                for (int j = 18; j < 24; ++j) {
                    const int r = min(row0 + 8 * j + lrow, D - 1);
#pragma unroll
                    for (int bb = 0; bb < 2; ++bb) { const unsigned v = xs[bb * D + r]; const unsigned dup = v | (v << 16);
                        if (j & 1) ag[bb] = mfma(Wf[j], BDIAG(dup), ag[bb]); else af[bb] = mfma(Wf[j], BDIAG(dup), af[bb]); }
                }
            }
            af[0] += ag[0]; af[1] += ag[1];
            T = __builtin_amdgcn_readfirstlane(ep) + 1u;
            float* scr = (float*)(lds + L_SCR + sw * 1024);
            if ((lane >> 5) == ((lane >> 3) & 1)) {
                const int r1 = lane >> 5, cb = 32 * ((lane >> 4) & 1) + (lane & 7);
#pragma unroll
                for (int bb = 0; bb < 2; ++bb)
#pragma unroll
                    for (int r = 0; r < 4; ++r) scr[(r1 * 2 + bb) * 64 + cb + 8 * r] = af[bb][r];
            }
            __builtin_amdgcn_fence(__ATOMIC_ACQ_REL, "wavefront");
            asm volatile("" ::: "memory");
            {
                const int bb = lane >> 5, pr = lane & 31;
                const float2 t0 = *(const float2*)&scr[bb * 64 + 2 * pr], t1 = *(const float2*)&scr[(2 + bb) * 64 + 2 * pr];
                const int off = sw == 0 ? WS_QP + (((kvh * 2 + bb) * 4 + kq) * 128 + cq * 32 + pr) * 16
                                        : (has_w1 ? WS_HP + (((c1 * 2 + bb) * 3 + k1) * 32 + pr) * 16 : OOB);
                STSC(mkpair(t0.x + t1.x, t0.y + t1.y, T), rws, off);
            }
            if (sw == 0) {
                const int qoff = WS_QP + ((kvh * 2 + b) * 4 * 128 + lane) * 16;
                v4u gq[8]; bool ok; unsigned spin = 0;
                NOUNROLL do {
#pragma unroll
                    for (int i = 0; i < 8; ++i) gq[i] = LDSC(rws, qoff + (i >> 1) * 128 * 16 + (i & 1) * 64 * 16);
                    ok = true;
#pragma unroll
                    for (int i = 0; i < 8; ++i) ok &= tag_ok(gq[i], T);
                    if (__all(ok)) break;
                    SPIN_PAUSE(1);
                } while (++spin < SPIN_CAP);
#pragma unroll
                for (int hf2 = 0; hf2 < 2; ++hf2) {
                    const float q0 = (__uint_as_float(gq[hf2].x) + __uint_as_float(gq[2 + hf2].x)) + (__uint_as_float(gq[4 + hf2].x) + __uint_as_float(gq[6 + hf2].x));
                    const float q1 = (__uint_as_float(gq[hf2].z) + __uint_as_float(gq[2 + hf2].z)) + (__uint_as_float(gq[4 + hf2].z) + __uint_as_float(gq[6 + hf2].z));
                    ((unsigned*)(lds + L_Q))[hf2 * 64 + lane] = cvtpk(q0, q1);
                }
                if (lane == 0) qflag[1] = T;
                asm volatile("s_waitcnt lgkmcnt(0)" ::: "memory");
                if (lane == 0) *qflag = 1u;
            }
            __builtin_amdgcn_s_setprio(0);
        }
        __builtin_amdgcn_sched_barrier(0);
        asm volatile("" :: "v"(pf3));
        { unsigned spin = 0; NOUNROLL while (qflag[5] < 8u && ++spin < (SPIN_CAP << 4)) __builtin_amdgcn_s_sleep(2); }
        v4u Wt[12];
#pragma unroll
        for (int j = 0; j < 12; ++j) { const int u = sw + 4 * j; Wt[j] = LDNT(rt, (8 * u < trows) ? ((trows * trg + 8 * u + lrow) * D + 64 * tcg) * 2 + lcol : OOB); }
        __builtin_amdgcn_sched_barrier(0);
        if (sw >= 2 || (sw == 1 && !has_w1)) {
            unsigned spin = 0; NOUNROLL while (*qflag == 0u && ++spin < (SPIN_CAP << 4)) __builtin_amdgcn_s_sleep(4);
            T = qflag[1];
        }
        if (role_w2) {
            const int st = tid - 512;
            const __amdgpu_buffer_rsrc_t rb1 = RSRC(b1, F * 2);
            v4u gh[2][3]; unsigned b1v[2]; int hoff[2];
#pragma unroll
            for (int e = 0; e < 2; ++e) {
                const int it = st + 256 * e; const bool valid = it < 384;
                const int hbb = it / 192, hpr = it % 192, hf = 384 * trg + 2 * hpr;
                hoff[e] = valid ? WS_HP + ((((hf >> 6) * 2 + hbb) * 3) * 32 + ((hf & 63) >> 1)) * 16 : OOB;
                b1v[e] = __builtin_amdgcn_raw_buffer_load_b32(rb1, valid ? hf * 2 : OOB, 0, 0);
            }
            bool ok; unsigned spin = 0;
            NOUNROLL do {
                ok = true;
#pragma unroll
                for (int e = 0; e < 2; ++e)
#pragma unroll
                    for (int i = 0; i < 3; ++i) { gh[e][i] = LDSC(rws, hoff[e] == OOB ? OOB : hoff[e] + i * 32 * 16); }
#pragma unroll
                for (int e = 0; e < 2; ++e)
#pragma unroll
                    for (int i = 0; i < 3; ++i) ok &= (hoff[e] == OOB) | tag_ok(gh[e][i], T);
                if (__all(ok)) break;
                SPIN_PAUSE(2);
            } while (++spin < SPIN_CAP);
#pragma unroll
            for (int e = 0; e < 2; ++e) {
                const int it = st + 256 * e;
                float h0 = __uint_as_float(gh[e][0].x) + __uint_as_float(gh[e][1].x) + __uint_as_float(gh[e][2].x);
                float h1 = __uint_as_float(gh[e][0].z) + __uint_as_float(gh[e][1].z) + __uint_as_float(gh[e][2].z);
                h0 = rbf(rbf(h0) + bf2f(b1v[e] & 0xffffu)); h1 = rbf(rbf(h1) + __uint_as_float(b1v[e] & 0xffff0000u));
                h0 = h0 > 0.f ? h0 : 0.f; h1 = h1 > 0.f ? h1 : 0.f;
                h0 *= h0; h1 *= h1;
                const int hbb = it / 192, hpr = it % 192;
                if (it < 384) { unsigned* act = (unsigned*)(lds + L_ACT); act[(2 * hpr) * 2 + hbb] = cvtpk(h0, h0); act[(2 * hpr + 1) * 2 + hbb] = cvtpk(h1, h1); }
            }
            asm volatile("s_waitcnt lgkmcnt(0)" ::: "memory");
            if (lane == 0) __hip_atomic_fetch_or((LAS unsigned*)(qflag + 3), 1u << sw, __ATOMIC_RELAXED, __HIP_MEMORY_SCOPE_WORKGROUP);
        }
        if (role_wo || role_w2) {
            { unsigned spin = 0; NOUNROLL while (qflag[3] != 15u && ++spin < (SPIN_CAP << 4)) __builtin_amdgcn_s_sleep(0); }
            asm volatile("" ::: "memory");
            const unsigned* act = (const unsigned*)(lds + L_ACT);
            f32x4 at[2][2] = {{{0, 0, 0, 0}, {0, 0, 0, 0}}, {{0, 0, 0, 0}, {0, 0, 0, 0}}};
            typedef unsigned v2u __attribute__((ext_vector_type(2)));
            v2u ad[8];
#pragma unroll
            for (int j = 0; j < 8; ++j) ad[j] = *(const v2u*)&act[(8 * (sw + 4 * j) + lrow) * 2];
#pragma unroll
            for (int j = 0; j < 8; ++j) { at[0][j & 1] = mfma(Wt[j], BDIAG(ad[j].x), at[0][j & 1]); at[1][j & 1] = mfma(Wt[j], BDIAG(ad[j].y), at[1][j & 1]); }
            if (role_w2) {
#pragma unroll
                for (int j = 8; j < 12; ++j) { const v2u a2 = *(const v2u*)&act[(8 * (sw + 4 * j) + lrow) * 2]; at[0][j & 1] = mfma(Wt[j], BDIAG(a2.x), at[0][j & 1]); at[1][j & 1] = mfma(Wt[j], BDIAG(a2.y), at[1][j & 1]); }
            }
            at[0][0] += at[0][1]; at[1][0] += at[1][1];
            float* red2 = (float*)(lds + L_RED2);
            if ((lane >> 5) == ((lane >> 3) & 1)) {
                const int r1 = lane >> 5, cb = 32 * ((lane >> 4) & 1) + (lane & 7);
#pragma unroll
                for (int bb = 0; bb < 2; ++bb)
#pragma unroll
                    for (int r = 0; r < 4; ++r) red2[((sw * 2 + r1) * 2 + bb) * 64 + cb + 8 * r] = at[bb][0][r];
            }
            asm volatile("s_waitcnt lgkmcnt(0)" ::: "memory");
            unsigned arr = 0;
            if (lane == 0) arr = __hip_atomic_fetch_add((LAS unsigned*)(qflag + 4), 1u, __ATOMIC_RELAXED, __HIP_MEMORY_SCOPE_WORKGROUP);
            arr = __builtin_amdgcn_readfirstlane(arr);
            asm volatile("" ::: "memory");
            if (arr == 3u) {
                const int bb = lane >> 5, pr = lane & 31;
                float v0 = 0.f, v1 = 0.f;
#pragma unroll
                for (int i = 0; i < 8; ++i) { const float2 t = *(const float2*)&red2[(i * 2 + bb) * 64 + 2 * pr]; v0 += t.x; v1 += t.y; }
                const int off = role_wo ? WS_SP + (((tcg * 2 + bb) * 16 + trg) * 32 + pr) * 16 : WS_MP + (((tcg * 2 + bb) * 12 + trg) * 32 + pr) * 16;
                STSC(mkpair(v0, v1, T), rws, off);
            }
        }
        asm volatile("1:");
    }

    if (bid >= 144 && bid < 216 && w == 1) {
        const int u = bid - 144, cg = u >> 3, fb = (u >> 2) & 1, q4 = u & 3;
        const int p8 = lane >> 3, j = lane & 7;
        const int col = 64 * cg + 16 * q4 + 2 * j;
        const unsigned xw = *(const unsigned*)(x + fb * D + col), bw = *(const unsigned*)(b2 + col);
        const int offs = WS_SP + (((cg * 2 + fb) * 16 + p8) * 32 + 8 * q4 + j) * 16;
        const int offm = WS_MP + (((cg * 2 + fb) * 12 + p8) * 32 + 8 * q4 + j) * 16;
        v4u g[4]; bool ok; unsigned spin = 0;
        NOUNROLL do {
            g[0] = LDSC(rws, offs); g[1] = LDSC(rws, offs + 8 * 32 * 16); g[2] = LDSC(rws, offm); g[3] = LDSC(rws, p8 < 4 ? offm + 8 * 32 * 16 : OOB);
            ok = tag_ok(g[0], T) & tag_ok(g[1], T) & tag_ok(g[2], T) & ((p8 >= 4) | tag_ok(g[3], T));
            if (__all(ok)) break;
            SPIN_PAUSE(1);
        } while (++spin < SPIN_CAP);
        float s0 = __uint_as_float(g[0].x) + __uint_as_float(g[1].x), s1 = __uint_as_float(g[0].z) + __uint_as_float(g[1].z);
        float m0 = __uint_as_float(g[2].x) + __uint_as_float(g[3].x), m1 = __uint_as_float(g[2].z) + __uint_as_float(g[3].z);
        s0 += dpp<0x128>(s0); s1 += dpp<0x128>(s1); m0 += dpp<0x128>(m0); m1 += dpp<0x128>(m1);
        s0 = xrow_sum(s0); s1 = xrow_sum(s1); m0 = xrow_sum(m0); m1 = xrow_sum(m1);
        if (lane < 8) {
            const float mv0 = rbf(rbf(m0) + bf2f(bw & 0xffffu)), mv1 = rbf(rbf(m1) + __uint_as_float(bw & 0xffff0000u));
            const float g0 = rbf(mv0 / (1.f + __expf(-mv0))), g1 = rbf(mv1 / (1.f + __expf(-mv1)));
            *(unsigned*)(out + fb * D + col) = cvtpk(bf2f(xw & 0xffffu) + rbf(rbf(s0) * g0), __uint_as_float(xw & 0xffff0000u) + rbf(rbf(s1) * g1));
        }
        if (u == 0 && lane == 0) __builtin_amdgcn_raw_buffer_store_b32(T, rws, WS_E, 0, 16);
    }
}

extern "C" void kernel_launch(void* const* d_in, const int* in_sizes, int n_in, void* d_out, int out_size, void* d_ws, size_t ws_size, hipStream_t stream) {
    fused_kernel<<<256, 768, 0, stream>>>((const u16*)d_in[0], (const u16*)d_in[1], (const u16*)d_in[2], (const u16*)d_in[3], (const u16*)d_in[4],
                                          (const u16*)d_in[6], (const u16*)d_in[7], (const u16*)d_in[8], (const u16*)d_in[9], (u16*)d_out, (unsigned char*)d_ws);
}
```

```cpp
#include <hip/hip_runtime.h>
#include <stdint.h>

typedef unsigned short u16;
typedef unsigned v4u __attribute__((ext_vector_type(4)));
typedef unsigned v2u __attribute__((ext_vector_type(2)));
typedef float f32x4 __attribute__((ext_vector_type(4)));
typedef short bf16x8 __attribute__((ext_vector_type(8)));
typedef __bf16 bf16x2 __attribute__((ext_vector_type(2)));
#define LAS __attribute__((address_space(3)))

constexpr int D = 576, HKV = 16, SK = 16384, DH = 128, WIN = 4096, F = 4608;
constexpr int WS_E = 0;
constexpr int WS_QP = 4096;
constexpr int WS_HP = WS_QP + 16 * 2 * 4 * 128 * 16;
constexpr int WS_AP = WS_HP + 72 * 2 * 3 * 32 * 16;
constexpr int AP_REC = 136 * 16;
constexpr int WS_SP = WS_AP + 256 * AP_REC;
constexpr int WS_MP = WS_SP + 9 * 2 * 16 * 32 * 16;
constexpr int WS_END = WS_MP + 9 * 2 * 12 * 32 * 16;
#define NOUNROLL _Pragma("clang loop unroll(disable)")
constexpr unsigned SPIN_CAP = 1u << 18;
constexpr int OOB = 0x7ffffff0;

#define RSRC(p, n) __builtin_amdgcn_make_buffer_rsrc((void*)(p), 0, (n), 0x00020000)
#define LDNT(rs, off) __builtin_amdgcn_raw_buffer_load_b128(rs, off, 0, 2)
#define LDSC(rs, off) __builtin_amdgcn_raw_buffer_load_b128(rs, off, 0, 16)
#define STSC(v, rs, off) __builtin_amdgcn_raw_buffer_store_b128(v, rs, off, 0, 16)

__device__ __forceinline__ float bf2f(unsigned b) { return __uint_as_float(b << 16); }
typedef float f32x2 __attribute__((ext_vector_type(2)));
__device__ __forceinline__ unsigned cvtpk(float lo, float hi) { const f32x2 v = {lo, hi}; return __builtin_bit_cast(unsigned, __builtin_convertvector(v, bf16x2)); }
__device__ __forceinline__ float rbf(float f) { return __uint_as_float(cvtpk(f, f) << 16); }
template <int CTRL> __device__ __forceinline__ float dpp(float x) {
    return __builtin_bit_cast(float, __builtin_amdgcn_mov_dpp(__builtin_bit_cast(int, x), CTRL, 0xf, 0xf, true));
}
__device__ __forceinline__ f32x4 mfma(v4u a, v4u b, f32x4 c) {
    return __builtin_amdgcn_mfma_f32_16x16x32_bf16(__builtin_bit_cast(bf16x8, a), __builtin_bit_cast(bf16x8, b), c, 0, 0, 0);
}
__device__ __forceinline__ float xrow_sum(float x) {
    auto s = __builtin_amdgcn_permlane16_swap(__float_as_uint(x), __float_as_uint(x), false, false);
    x = __uint_as_float(s[0]) + __uint_as_float(s[1]);
    auto t = __builtin_amdgcn_permlane32_swap(__float_as_uint(x), __float_as_uint(x), false, false);
    return __uint_as_float(t[0]) + __uint_as_float(t[1]);
}
__device__ __forceinline__ float xrow_max(float x) {
    auto s = __builtin_amdgcn_permlane16_swap(__float_as_uint(x), __float_as_uint(x), false, false);
    x = fmaxf(__uint_as_float(s[0]), __uint_as_float(s[1]));
    auto t = __builtin_amdgcn_permlane32_swap(__float_as_uint(x), __float_as_uint(x), false, false);
    return fmaxf(__uint_as_float(t[0]), __uint_as_float(t[1]));
}
__device__ __forceinline__ float wave_sum(float v) {
    v += dpp<0xB1>(v); v += dpp<0x4E>(v); v += dpp<0x141>(v); v += dpp<0x140>(v);
    return xrow_sum(v);
}
__device__ __forceinline__ bool tag_ok(v4u p, unsigned T) { return (p.y == T) & (p.w == ~T); }
__device__ __forceinline__ unsigned tag_bad(v4u p, unsigned T) { return (p.y ^ T) | (p.w ^ ~T); }
__device__ __forceinline__ void rbf2(float& a, float& b) { const unsigned p = cvtpk(a, b); a = __uint_as_float(p << 16); b = __uint_as_float(p & 0xffff0000u); }
__device__ __forceinline__ float rcp(float x) { return __builtin_amdgcn_rcpf(x); }

__device__ __forceinline__ v4u mkpair(float a, float b, unsigned T) { return v4u{__float_as_uint(a), T, __float_as_uint(b), ~T}; }

#define SPIN_PAUSE(n) do { __builtin_amdgcn_s_sleep(n); asm volatile("" :: "s"(ws) : "memory"); } while (0)

constexpr int L_XS = 0;
constexpr int L_Q = 4608;
constexpr int L_ACT = 13312 + 8192;
constexpr int L_ML = 6656;
constexpr int L_FLAG = 7040;
constexpr int L_SCR = 7168;
constexpr int L_RED2 = 9216;
constexpr int L_RED = 13312;
constexpr int L_K = L_RED + 16384;
constexpr int L_BYTES = L_K + 131072;

__global__ void __launch_bounds__(768, 3)
fused_kernel(const u16* __restrict__ x, const u16* __restrict__ kc, const u16* __restrict__ vc, const u16* __restrict__ wq, const u16* __restrict__ wo,
             const u16* __restrict__ w1, const u16* __restrict__ b1, const u16* __restrict__ w2, const u16* __restrict__ b2,
             u16* __restrict__ out, unsigned char* ws) {
    __shared__ __attribute__((aligned(16))) unsigned char lds[L_BYTES];
    const int tid = threadIdx.x, lane = tid & 63, w = __builtin_amdgcn_readfirstlane(tid >> 6), bid = blockIdx.x;
#define KARGS asm volatile("" :: "s"(x), "s"(kc), "s"(vc), "s"(wq), "s"(wo), "s"(w1), "s"(b1), "s"(w2), "s"(b2), "s"(out), "s"(ws))
    unsigned pf3;
    {
        unsigned long long pc0; unsigned len;
        asm volatile("s_getpc_b64 %0\n2:\n\ts_mov_b32 %1, 1f-2b" : "=s"(pc0), "=s"(len));
        const __amdgpu_buffer_rsrc_t rc = RSRC(pc0 & ~255ull, len + ((unsigned)pc0 & 255u));
        pf3 = __builtin_amdgcn_raw_buffer_load_b32(rc, tid * 32, 0, 0);
    }
    __builtin_amdgcn_sched_barrier(0);
    volatile LAS unsigned* qflag = (volatile LAS unsigned*)(LAS unsigned char*)(lds + L_FLAG);
    if (tid < 8) qflag[tid] = 0u;
    const int s = bid & 7, b = (bid >> 3) & 1, kvh = bid >> 4;
    const int lrow = lane >> 3, lcol = (lane & 7) * 16;
    const __amdgpu_buffer_rsrc_t rws = RSRC(ws, WS_END);
    int ka0 = lane * 16, ka1 = ka0 + 4096, ka2 = ka0 + 8192, ka3 = ka0 + 12288;
    const unsigned kvo = (unsigned)(((b * HKV + kvh) * SK + (SK - WIN) + s * 512 + (w & 7) * 64) * (DH * 2));
    asm volatile("" : "+v"(ka0), "+v"(ka1), "+v"(ka2), "+v"(ka3));
    KARGS;
#define FRONT_LOADS \
    if (w == 8 || w == 9) { \
        __builtin_amdgcn_s_setprio(3); \
        const __amdgpu_buffer_rsrc_t rx0 = RSRC(x, D * 2), rx1 = RSRC(x + D, D * 2); \
        const int e_k1 = bid / 72, e_c1 = bid - 72 * e_k1; \
        const bool q8 = w == 8; \
        const int ld = q8 ? 4096 * 2 : F * 2; \
        const int colb = q8 ? 512 * kvh + 128 * (bid & 3) : 128 * e_c1; \
        const int rowb = q8 ? 144 * ((bid & 15) >> 2) : 192 * e_k1; \
        const int nrec = q8 ? 144 * 4096 * 2 : (bid < 216 ? 192 * F * 2 : 0); \
        const __amdgpu_buffer_rsrc_t rw = RSRC((const char*)(q8 ? wq : w1) + (unsigned)(rowb * ld), nrec); \
_Pragma("unroll") \
        for (int j = 0; j < 5; ++j) { xr[0][j] = __builtin_amdgcn_raw_buffer_load_b32(rx0, lane * 4 + 256 * j, 0, 0); xr[1][j] = __builtin_amdgcn_raw_buffer_load_b32(rx1, lane * 4 + 256 * j, 0, 0); } \
        int off = lrow * ld + lcol + colb; const int step = 8 * ld; \
_Pragma("unroll") \
        for (int j = 0; j < 24; ++j) { Wf[j] = LDNT(rw, off); off += step; asm volatile("" : "+v"(off)); } \
        ep = __builtin_amdgcn_raw_buffer_load_b32(rws, WS_E, 0, 16); \
    }
    unsigned xr[2][5]; v4u Wf[24];
    unsigned ep = 0;
    FRONT_LOADS
    __builtin_amdgcn_sched_barrier(0);
    __syncthreads();
    __builtin_amdgcn_sched_barrier(0);
#define ROLES \
    int bi2 = bid; asm volatile("" : "+s"(bi2)); \
    int ln2 = lane; asm volatile("" : "+v"(ln2)); \
    const bool role_wo = bi2 < 144, role_w2 = bi2 >= 144 && bi2 < 252; \
    const int u2 = role_wo ? bi2 : bi2 - 144; \
    const int trg = u2 / 9, tcg = u2 - 9 * trg;            \
    const unsigned hm = (ln2 & 1) ? 0xffff0000u : 0x0000ffffu;        \
    const int dsel = (ln2 >> 1) & 3; \
    const unsigned M0 = dsel == 0 ? hm : 0u, M1 = dsel == 1 ? hm : 0u, M2 = dsel == 2 ? hm : 0u, M3 = dsel == 3 ? hm : 0u;
#define BDIAG(dup) v4u{(dup) & M0, (dup) & M1, (dup) & M2, (dup) & M3}
    unsigned T;

    if (__builtin_expect(w < 8, 1)) {
        v4u K[16], V[16];
        {
            const __amdgpu_buffer_rsrc_t rk = RSRC((const char*)kc + kvo, 64 * 256);
            const __amdgpu_buffer_rsrc_t rv = RSRC((const char*)vc + kvo, 64 * 256);
            __builtin_amdgcn_s_setprio(1);
#pragma unroll
            for (int j = 0; j < 16; ++j) K[j] = LDNT(rk, (j < 4 ? ka0 : j < 8 ? ka1 : j < 12 ? ka2 : ka3) + (j & 3) * 1024);
            __builtin_amdgcn_sched_barrier(0);
            __builtin_amdgcn_s_setprio(0);
            __builtin_amdgcn_sched_barrier(0);
#pragma unroll
            for (int j = 0; j < 16; ++j) V[j] = LDNT(rv, (j < 4 ? ka0 : j < 8 ? ka1 : j < 12 ? ka2 : ka3) + (j & 3) * 1024);
        }
        __builtin_amdgcn_sched_barrier(0);
        ROLES
        if (lane == 0) __hip_atomic_fetch_add((LAS unsigned*)(qflag + 5), 1u, __ATOMIC_RELAXED, __HIP_MEMORY_SCOPE_WORKGROUP);
        const int g = lane >> 4, c = lane & 15;
#pragma unroll
        for (int j = 0; j < 16; ++j) { const int i = 4 * (j & 3) + g; *(v4u*)(lds + L_K + w * 16384 + (j >> 2) * 4096 + i * 256 + ((c ^ (4 * g + (j & 3))) * 16)) = K[j]; }
        asm volatile("" :: "v"(pf3));
        { unsigned spin = 0; NOUNROLL while (*qflag == 0u && ++spin < (SPIN_CAP << 4)) __builtin_amdgcn_s_sleep(1); T = qflag[1]; }
        asm volatile("s_waitcnt lgkmcnt(0)" ::: "memory");
        v4u qf[4];
#pragma unroll
        for (int ks = 0; ks < 4; ++ks) qf[ks] = *(const v4u*)(lds + L_Q + (c >> 3) * 256 + (4 * ks + g) * 16);
        f32x4 sc[4];
#pragma unroll
        for (int t = 0; t < 4; ++t) {
            sc[t] = f32x4{0, 0, 0, 0};
#pragma unroll
            for (int ks = 0; ks < 4; ++ks) {
                const v4u ka = *(const v4u*)(lds + L_K + w * 16384 + t * 4096 + (4 * (c & 3) + (c >> 2)) * 256 + (((4 * ks + g) ^ c) * 16));
                sc[t] = mfma(ka, qf[ks], sc[t]);
            }
        }
        float m = -INFINITY;
#pragma unroll
        for (int t = 0; t < 4; ++t)
#pragma unroll
            for (int r = 0; r < 4; ++r) { sc[t][r] = rbf(rbf(sc[t][r]) * 0.088388347648318440f); m = fmaxf(m, sc[t][r]); }
        m = xrow_max(m);
        float l = 0.f;
        constexpr float LOG2E = 1.4426950408889634f;
        const float mm = m * LOG2E;
        unsigned pd[16];
#pragma unroll
        for (int t = 0; t < 4; ++t)
#pragma unroll
            for (int r = 0; r < 4; ++r) { const float p = __builtin_amdgcn_exp2f(sc[t][r] * LOG2E - mm); l += p; pd[4 * t + r] = cvtpk(p, p); }
        l = xrow_sum(l);
        f32x4 acc0 = {0, 0, 0, 0}, acc1 = {0, 0, 0, 0};
#pragma unroll
        for (int t = 0; t < 16; t += 2) { acc0 = mfma(V[t], BDIAG(pd[t]), acc0); acc1 = mfma(V[t + 1], BDIAG(pd[t + 1]), acc1); }
        acc0 += acc1;
        __builtin_amdgcn_s_setprio(3);
        const int hh = (lane >> 3) & 1;
        *(f32x4*)(lds + L_RED + (w * 64 + lane) * 16) = acc0;
        if ((lane & 0x37) == 0) *(float2*)(lds + L_ML + (w * 2 + hh) * 8) = make_float2(mm, l);
        asm volatile("s_waitcnt lgkmcnt(0)" ::: "memory");
        unsigned arr = 0;
        if (lane == 0) arr = __hip_atomic_fetch_add((LAS unsigned*)(qflag + 2), 1u, __ATOMIC_RELAXED, __HIP_MEMORY_SCOPE_WORKGROUP);
        arr = __builtin_amdgcn_readfirstlane(arr);
        asm volatile("" ::: "memory");
        if (arr == 7u) {
            float2 ml[8]; f32x4 pa[8];
#pragma unroll
            for (int i = 0; i < 8; ++i) { ml[i] = *(const float2*)(lds + L_ML + (i * 2 + hh) * 8); pa[i] = *(const f32x4*)(lds + L_RED + (i * 64 + lane) * 16); }
            float M = ml[0].x;
#pragma unroll
            for (int i = 1; i < 8; ++i) M = fmaxf(M, ml[i].x);
            f32x4 a = {0, 0, 0, 0}; float L = 0.f;
#pragma unroll
            for (int i = 0; i < 8; ++i) { const float wt = __builtin_amdgcn_exp2f(ml[i].x - M); a += wt * pa[i]; L += wt * ml[i].y; }
            const int off = WS_AP + bid * AP_REC + lane * 16;
            STSC(mkpair(a[0], a[1], T), rws, off);
            STSC(mkpair(a[2], a[3], T), rws, off + 1024);
            STSC(mkpair(M, L, T), rws, (lane & 0x37) == 0 ? WS_AP + bid * AP_REC + (128 + hh) * 16 : OOB);
            if (lane == 0) qflag[6] = 1u;
        }
        if (role_wo && w < 4) {
            { unsigned spin = 0; NOUNROLL while (qflag[6] == 0u && ++spin < (SPIN_CAP << 4)) __builtin_amdgcn_s_sleep(2); }
            const int bb = (w >> 1) & 1, h2 = w & 1;
            const unsigned bit = 1u << (w & 3);
            const int base = WS_AP + (trg * 16 + bb * 8) * AP_REC;
            v4u g[8], gm[8];
            bool ok = false;
            unsigned spin = 0;
            NOUNROLL do {
#pragma unroll
                for (int i = 0; i < 8; ++i) { g[i] = LDSC(rws, base + i * AP_REC + ((lane >> 5) * 64 + ((lane >> 3) & 3) * 16 + h2 * 8 + (lane & 7)) * 16); gm[i] = LDSC(rws, base + i * AP_REC + (128 + h2) * 16); }
                bool o = true;
#pragma unroll
                for (int i = 0; i < 8; ++i) o &= tag_ok(g[i], T) & tag_ok(gm[i], T);
                ok = __all(o);
                if (ok) break;
                SPIN_PAUSE(2);
            } while (++spin < SPIN_CAP);
            if (ok) {
                float M = __uint_as_float(gm[0].x);
#pragma unroll
                for (int i = 1; i < 8; ++i) M = fmaxf(M, __uint_as_float(gm[i].x));
                float a0 = 0.f, a1 = 0.f, L = 0.f;
#pragma unroll
                for (int i = 0; i < 8; ++i) {
                    const float wt = __builtin_amdgcn_exp2f(__uint_as_float(gm[i].x) - M);
                    a0 += wt * __uint_as_float(g[i].x); a1 += wt * __uint_as_float(g[i].z); L += wt * __uint_as_float(gm[i].z);
                }
                const float ri = 1.f / L;
                const int dl = h2 * 128 + 32 * ((lane >> 3) & 3) + 16 * (lane >> 5) + (lane & 7);
                unsigned* act = (unsigned*)(lds + L_ACT);
                act[dl * 2 + bb] = cvtpk(a0 * ri, a0 * ri); act[(dl + 8) * 2 + bb] = cvtpk(a1 * ri, a1 * ri);
                asm volatile("s_waitcnt lgkmcnt(0)" ::: "memory");
                if (lane == 0) __hip_atomic_fetch_or((LAS unsigned*)(qflag + 3), bit, __ATOMIC_RELAXED, __HIP_MEMORY_SCOPE_WORKGROUP);
            } else if (spin >= SPIN_CAP) {
                if (lane == 0) __hip_atomic_fetch_or((LAS unsigned*)(qflag + 3), bit, __ATOMIC_RELAXED, __HIP_MEMORY_SCOPE_WORKGROUP);
            }
        }
    } else {
        const int sw = w - 8;
        ROLES
        const int kq = (bi2 & 15) >> 2, cq = bi2 & 3;
        const bool has_w1 = bi2 < 216;
        const int k1 = bi2 / 72, c1 = bi2 - 72 * k1;
        const int trows = role_wo ? 256 : 384;
        const __amdgpu_buffer_rsrc_t rt = RSRC(role_wo ? wo : w2, role_wo ? 4096 * D * 2 : (role_w2 ? F * D * 2 : 0));
        if (sw < 2) {
            const int row0 = sw == 0 ? 144 * kq : 192 * k1;
            {
                float xv[2][10], nv[2][10];
#pragma unroll
                for (int bb = 0; bb < 2; ++bb) {
                    float t = 0.f;
#pragma unroll
                    for (int j = 0; j < 5; ++j) { xv[bb][2 * j] = bf2f(xr[bb][j] & 0xffffu); xv[bb][2 * j + 1] = __uint_as_float(xr[bb][j] & 0xffff0000u); t += xv[bb][2 * j] + xv[bb][2 * j + 1]; }
                    const float mean = wave_sum(t) * (1.f / D);
                    float t8 = 0.f; t = 0.f;
#pragma unroll
                    for (int k = 0; k < 8; ++k) { const float d = xv[bb][k] - mean; t += d * d; }
#pragma unroll
                    for (int k = 8; k < 10; ++k) { const float d = xv[bb][k] - mean; t8 += d * d; }
                    const float var = wave_sum(lane < 32 ? t + t8 : t) * (1.f / D);
                    const float mb = rbf(mean);
                    const float ri = rcp(rbf(__builtin_amdgcn_sqrtf(rbf(rbf(var) + 1e-5f))));
#pragma unroll
                    for (int k = 0; k < 10; k += 2) { float a0 = xv[bb][k] - mb, a1 = xv[bb][k + 1] - mb; rbf2(a0, a1); nv[bb][k] = a0 * ri; nv[bb][k + 1] = a1 * ri; }
                }
#pragma unroll
                for (int j = 0; j < 5; ++j) {
                    const v4u o = {cvtpk(nv[0][2 * j], nv[0][2 * j]), cvtpk(nv[1][2 * j], nv[1][2 * j]), cvtpk(nv[0][2 * j + 1], nv[0][2 * j + 1]), cvtpk(nv[1][2 * j + 1], nv[1][2 * j + 1])};
                    if (j < 4 || lane < 32) *(v4u*)(lds + L_XS + (lane + 64 * j) * 16) = o;
                }
            }
            __builtin_amdgcn_fence(__ATOMIC_ACQ_REL, "wavefront");
            asm volatile("" ::: "memory");
            const v2u* xs = (const v2u*)(lds + L_XS) + row0 + lrow;
            f32x4 af[2] = {{0, 0, 0, 0}, {0, 0, 0, 0}}, ag[2] = {{0, 0, 0, 0}, {0, 0, 0, 0}};
#pragma unroll
            for (int j = 0; j < 18; ++j) {
                const v2u d = xs[8 * j];
                if (j & 1) { ag[0] = mfma(Wf[j], BDIAG(d.x), ag[0]); ag[1] = mfma(Wf[j], BDIAG(d.y), ag[1]); } else { af[0] = mfma(Wf[j], BDIAG(d.x), af[0]); af[1] = mfma(Wf[j], BDIAG(d.y), af[1]); }
            }
            if (sw == 1) {
#pragma unroll
                for (int j = 18; j < 24; ++j) {
                    const v2u d = xs[8 * j];
                    if (j & 1) { ag[0] = mfma(Wf[j], BDIAG(d.x), ag[0]); ag[1] = mfma(Wf[j], BDIAG(d.y), ag[1]); } else { af[0] = mfma(Wf[j], BDIAG(d.x), af[0]); af[1] = mfma(Wf[j], BDIAG(d.y), af[1]); }
                }
            }
            af[0] += ag[0]; af[1] += ag[1];
            T = __builtin_amdgcn_readfirstlane(ep) + 1u;
            float* scr = (float*)(lds + L_SCR + sw * 1024);
            if ((lane >> 5) == ((lane >> 3) & 1)) {
                const int r1 = lane >> 5, cb = 32 * ((lane >> 4) & 1) + (lane & 7);
#pragma unroll
                for (int bb = 0; bb < 2; ++bb)
#pragma unroll
                    for (int r = 0; r < 4; ++r) scr[(r1 * 2 + bb) * 64 + cb + 8 * r] = af[bb][r];
            }
            __builtin_amdgcn_fence(__ATOMIC_ACQ_REL, "wavefront");
            asm volatile("" ::: "memory");
            {
                const int bb = lane >> 5, pr = lane & 31;
                const float2 t0 = *(const float2*)&scr[bb * 64 + 2 * pr], t1 = *(const float2*)&scr[(2 + bb) * 64 + 2 * pr];
                const int off = sw == 0 ? WS_QP + (((kvh * 2 + bb) * 4 + kq) * 128 + cq * 32 + pr) * 16
                                        : (has_w1 ? WS_HP + (((c1 * 2 + bb) * 3 + k1) * 32 + pr) * 16 : OOB);
                STSC(mkpair(t0.x + t1.x, t0.y + t1.y, T), rws, off);
            }
            if (sw == 0) {
                const int qoff = WS_QP + ((kvh * 2 + b) * 4 * 128 + lane) * 16;
                v4u gq[8]; bool ok; unsigned spin = 0;
                NOUNROLL do {
#pragma unroll
                    for (int i = 0; i < 8; ++i) gq[i] = LDSC(rws, qoff + (i >> 1) * 128 * 16 + (i & 1) * 64 * 16);
                    ok = true;
#pragma unroll
                    for (int i = 0; i < 8; ++i) ok &= tag_ok(gq[i], T);
                    if (__all(ok)) break;
                    SPIN_PAUSE(1);
                } while (++spin < SPIN_CAP);
#pragma unroll
                for (int hf2 = 0; hf2 < 2; ++hf2) {
                    const float q0 = (__uint_as_float(gq[hf2].x) + __uint_as_float(gq[2 + hf2].x)) + (__uint_as_float(gq[4 + hf2].x) + __uint_as_float(gq[6 + hf2].x));
                    const float q1 = (__uint_as_float(gq[hf2].z) + __uint_as_float(gq[2 + hf2].z)) + (__uint_as_float(gq[4 + hf2].z) + __uint_as_float(gq[6 + hf2].z));
                    ((unsigned*)(lds + L_Q))[hf2 * 64 + lane] = cvtpk(q0, q1);
                }
                if (lane == 0) qflag[1] = T;
                asm volatile("s_waitcnt lgkmcnt(0)" ::: "memory");
                if (lane == 0) *qflag = 1u;
            }
            __builtin_amdgcn_s_setprio(0);
        }
        __builtin_amdgcn_sched_barrier(0);
        asm volatile("" :: "v"(pf3));
        { unsigned spin = 0; NOUNROLL while (qflag[5] < 8u && ++spin < (SPIN_CAP << 4)) __builtin_amdgcn_s_sleep(2); }
        v4u Wt[12];
#pragma unroll
        for (int j = 0; j < 12; ++j) { const int u = sw + 4 * j; Wt[j] = LDNT(rt, (8 * u < trows) ? ((trows * trg + 8 * u + lrow) * D + 64 * tcg) * 2 + lcol : OOB); }
        __builtin_amdgcn_sched_barrier(0);
        if (sw >= 2 || (sw == 1 && !has_w1)) {
            unsigned spin = 0; NOUNROLL while (*qflag == 0u && ++spin < (SPIN_CAP << 4)) __builtin_amdgcn_s_sleep(4);
            T = qflag[1];
        }
        if (role_w2) {
            const int st = tid - 512;
            const __amdgpu_buffer_rsrc_t rb1 = RSRC(b1, F * 2);
            v4u gh[2][3]; unsigned b1v[2]; int hoff[2];
#pragma unroll
            for (int e = 0; e < 2; ++e) {
                const int it = st + 256 * e; const bool valid = it < 384;
                const int hbb = it / 192, hpr = it % 192, hf = 384 * trg + 2 * hpr;
                hoff[e] = valid ? WS_HP + ((((hf >> 6) * 2 + hbb) * 3) * 32 + ((hf & 63) >> 1)) * 16 : OOB;
                b1v[e] = __builtin_amdgcn_raw_buffer_load_b32(rb1, valid ? hf * 2 : OOB, 0, 0);
            }
            bool ok; unsigned spin = 0;
            NOUNROLL do {
                ok = true;
#pragma unroll
                for (int e = 0; e < 2; ++e)
#pragma unroll
                    for (int i = 0; i < 3; ++i) { gh[e][i] = LDSC(rws, hoff[e] == OOB ? OOB : hoff[e] + i * 32 * 16); }
#pragma unroll
                for (int e = 0; e < 2; ++e)
#pragma unroll
                    for (int i = 0; i < 3; ++i) ok &= (hoff[e] == OOB) | tag_ok(gh[e][i], T);
                if (__all(ok)) break;
                SPIN_PAUSE(2);
            } while (++spin < SPIN_CAP);
#pragma unroll
            for (int e = 0; e < 2; ++e) {
                const int it = st + 256 * e;
                float h0 = __uint_as_float(gh[e][0].x) + __uint_as_float(gh[e][1].x) + __uint_as_float(gh[e][2].x);
                float h1 = __uint_as_float(gh[e][0].z) + __uint_as_float(gh[e][1].z) + __uint_as_float(gh[e][2].z);
                h0 = rbf(rbf(h0) + bf2f(b1v[e] & 0xffffu)); h1 = rbf(rbf(h1) + __uint_as_float(b1v[e] & 0xffff0000u));
                h0 = h0 > 0.f ? h0 : 0.f; h1 = h1 > 0.f ? h1 : 0.f;
                h0 *= h0; h1 *= h1;
                const int hbb = it / 192, hpr = it % 192;
                if (it < 384) { unsigned* act = (unsigned*)(lds + L_ACT); act[(2 * hpr) * 2 + hbb] = cvtpk(h0, h0); act[(2 * hpr + 1) * 2 + hbb] = cvtpk(h1, h1); }
            }
            asm volatile("s_waitcnt lgkmcnt(0)" ::: "memory");
            if (lane == 0) __hip_atomic_fetch_or((LAS unsigned*)(qflag + 3), 1u << sw, __ATOMIC_RELAXED, __HIP_MEMORY_SCOPE_WORKGROUP);
        }
        if (role_wo || role_w2) {
            { unsigned spin = 0; NOUNROLL while (qflag[3] != 15u && ++spin < (SPIN_CAP << 4)) __builtin_amdgcn_s_sleep(0); }
            asm volatile("" ::: "memory");
            const unsigned* act = (const unsigned*)(lds + L_ACT);
            f32x4 at[2][2] = {{{0, 0, 0, 0}, {0, 0, 0, 0}}, {{0, 0, 0, 0}, {0, 0, 0, 0}}};
            v2u ad[8];
#pragma unroll
            for (int j = 0; j < 8; ++j) ad[j] = *(const v2u*)&act[(8 * (sw + 4 * j) + lrow) * 2];
#pragma unroll
            for (int j = 0; j < 8; ++j) { at[0][j & 1] = mfma(Wt[j], BDIAG(ad[j].x), at[0][j & 1]); at[1][j & 1] = mfma(Wt[j], BDIAG(ad[j].y), at[1][j & 1]); }
            if (role_w2) {
#pragma unroll
                for (int j = 8; j < 12; ++j) { const v2u a2 = *(const v2u*)&act[(8 * (sw + 4 * j) + lrow) * 2]; at[0][j & 1] = mfma(Wt[j], BDIAG(a2.x), at[0][j & 1]); at[1][j & 1] = mfma(Wt[j], BDIAG(a2.y), at[1][j & 1]); }
            }
            at[0][0] += at[0][1]; at[1][0] += at[1][1];
            float* red2 = (float*)(lds + L_RED2);
            if ((lane >> 5) == ((lane >> 3) & 1)) {
                const int r1 = lane >> 5, cb = 32 * ((lane >> 4) & 1) + (lane & 7);
#pragma unroll
                for (int bb = 0; bb < 2; ++bb)
#pragma unroll
                    for (int r = 0; r < 4; ++r) red2[((sw * 2 + r1) * 2 + bb) * 64 + cb + 8 * r] = at[bb][0][r];
            }
            asm volatile("s_waitcnt lgkmcnt(0)" ::: "memory");
            unsigned arr = 0;
            if (lane == 0) arr = __hip_atomic_fetch_add((LAS unsigned*)(qflag + 4), 1u, __ATOMIC_RELAXED, __HIP_MEMORY_SCOPE_WORKGROUP);
            arr = __builtin_amdgcn_readfirstlane(arr);
            asm volatile("" ::: "memory");
            if (arr == 3u) {
                const int bb = lane >> 5, pr = lane & 31;
                float v0 = 0.f, v1 = 0.f;
#pragma unroll
                for (int i = 0; i < 8; ++i) { const float2 t = *(const float2*)&red2[(i * 2 + bb) * 64 + 2 * pr]; v0 += t.x; v1 += t.y; }
                const int off = role_wo ? WS_SP + (((tcg * 2 + bb) * 16 + trg) * 32 + pr) * 16 : WS_MP + (((tcg * 2 + bb) * 12 + trg) * 32 + pr) * 16;
                STSC(mkpair(v0, v1, T), rws, off);
            }
        }
        asm volatile("1:");
    }

    if (bid >= 144 && bid < 216 && w == 1) {
        const int u = bid - 144, cg = u >> 3, fb = (u >> 2) & 1, q4 = u & 3;
        const int p8 = lane >> 3, j = lane & 7;
        const int col = 64 * cg + 16 * q4 + 2 * j;
        const unsigned xw = *(const unsigned*)(x + fb * D + col), bw = *(const unsigned*)(b2 + col);
        const int offs = WS_SP + (((cg * 2 + fb) * 16 + p8) * 32 + 8 * q4 + j) * 16;
        const int offm = WS_MP + (((cg * 2 + fb) * 12 + p8) * 32 + 8 * q4 + j) * 16;
        v4u g[4]; bool ok; unsigned spin = 0;
        NOUNROLL do {
            g[0] = LDSC(rws, offs); g[1] = LDSC(rws, offs + 8 * 32 * 16); g[2] = LDSC(rws, offm); g[3] = LDSC(rws, p8 < 4 ? offm + 8 * 32 * 16 : OOB);
            ok = tag_ok(g[0], T) & tag_ok(g[1], T) & tag_ok(g[2], T) & ((p8 >= 4) | tag_ok(g[3], T));
            if (__all(ok)) break;
            SPIN_PAUSE(1);
        } while (++spin < SPIN_CAP);
        float s0 = __uint_as_float(g[0].x) + __uint_as_float(g[1].x), s1 = __uint_as_float(g[0].z) + __uint_as_float(g[1].z);
        float m0 = __uint_as_float(g[2].x) + __uint_as_float(g[3].x), m1 = __uint_as_float(g[2].z) + __uint_as_float(g[3].z);
        s0 += dpp<0x128>(s0); s1 += dpp<0x128>(s1); m0 += dpp<0x128>(m0); m1 += dpp<0x128>(m1);
        s0 = xrow_sum(s0); s1 = xrow_sum(s1); m0 = xrow_sum(m0); m1 = xrow_sum(m1);
        if (lane < 8) {
            const float mv0 = rbf(rbf(m0) + bf2f(bw & 0xffffu)), mv1 = rbf(rbf(m1) + __uint_as_float(bw & 0xffff0000u));
            const float g0 = rbf(mv0 / (1.f + __expf(-mv0))), g1 = rbf(mv1 / (1.f + __expf(-mv1)));
            *(unsigned*)(out + fb * D + col) = cvtpk(bf2f(xw & 0xffffu) + rbf(rbf(s0) * g0), __uint_as_float(xw & 0xffff0000u) + rbf(rbf(s1) * g1));
        }
        if (u == 0 && lane == 0) __builtin_amdgcn_raw_buffer_store_b32(T, rws, WS_E, 0, 16);
    }
}

extern "C" void kernel_launch(void* const* d_in, const int* in_sizes, int n_in, void* d_out, int out_size, void* d_ws, size_t ws_size, hipStream_t stream) {
    fused_kernel<<<256, 768, 0, stream>>>((const u16*)d_in[0], (const u16*)d_in[1], (const u16*)d_in[2], (const u16*)d_in[3], (const u16*)d_in[4],
                                          (const u16*)d_in[6], (const u16*)d_in[7], (const u16*)d_in[8], (const u16*)d_in[9], (u16*)d_out, (unsigned char*)d_ws);
}
```

```cpp
#include <hip/hip_runtime.h>
#include <stdint.h>

typedef unsigned short u16;
typedef unsigned v4u __attribute__((ext_vector_type(4)));
typedef unsigned v2u __attribute__((ext_vector_type(2)));
typedef float f32x4 __attribute__((ext_vector_type(4)));
typedef short bf16x8 __attribute__((ext_vector_type(8)));
typedef __bf16 bf16x2 __attribute__((ext_vector_type(2)));
#define LAS __attribute__((address_space(3)))

constexpr int D = 576, HKV = 16, SK = 16384, DH = 128, WIN = 4096, F = 4608;
constexpr int WS_E = 0;
constexpr int WS_QP = 4096;
constexpr int WS_HP = WS_QP + 16 * 2 * 4 * 128 * 16;
constexpr int WS_AP = WS_HP + 72 * 2 * 3 * 32 * 16;
constexpr int AP_REC = 136 * 16;
constexpr int WS_SP = WS_AP + 256 * AP_REC;
constexpr int WS_MP = WS_SP + 9 * 2 * 16 * 32 * 16;
constexpr int WS_END = WS_MP + 9 * 2 * 12 * 32 * 16;
#define NOUNROLL _Pragma("clang loop unroll(disable)")
constexpr unsigned SPIN_CAP = 1u << 18;
constexpr int OOB = 0x7ffffff0;

#define RSRC(p, n) __builtin_amdgcn_make_buffer_rsrc((void*)(p), 0, (n), 0x00020000)
#define LDNT(rs, off) __builtin_amdgcn_raw_buffer_load_b128(rs, off, 0, 2)
#define LDSC(rs, off) __builtin_amdgcn_raw_buffer_load_b128(rs, off, 0, 16)
#define STSC(v, rs, off) __builtin_amdgcn_raw_buffer_store_b128(v, rs, off, 0, 16)

__device__ __forceinline__ float bf2f(unsigned b) { return __uint_as_float(b << 16); }
typedef float f32x2 __attribute__((ext_vector_type(2)));
__device__ __forceinline__ unsigned cvtpk(float lo, float hi) { const f32x2 v = {lo, hi}; return __builtin_bit_cast(unsigned, __builtin_convertvector(v, bf16x2)); }
__device__ __forceinline__ float rbf(float f) { return __uint_as_float(cvtpk(f, f) << 16); }
template <int CTRL> __device__ __forceinline__ float dpp(float x) {
    return __builtin_bit_cast(float, __builtin_amdgcn_mov_dpp(__builtin_bit_cast(int, x), CTRL, 0xf, 0xf, true));
}
__device__ __forceinline__ f32x4 mfma(v4u a, v4u b, f32x4 c) {
    return __builtin_amdgcn_mfma_f32_16x16x32_bf16(__builtin_bit_cast(bf16x8, a), __builtin_bit_cast(bf16x8, b), c, 0, 0, 0);
}
__device__ __forceinline__ float xrow_sum(float x) {
    auto s = __builtin_amdgcn_permlane16_swap(__float_as_uint(x), __float_as_uint(x), false, false);
    x = __uint_as_float(s[0]) + __uint_as_float(s[1]);
    auto t = __builtin_amdgcn_permlane32_swap(__float_as_uint(x), __float_as_uint(x), false, false);
    return __uint_as_float(t[0]) + __uint_as_float(t[1]);
}
__device__ __forceinline__ float xrow_max(float x) {
    auto s = __builtin_amdgcn_permlane16_swap(__float_as_uint(x), __float_as_uint(x), false, false);
    x = fmaxf(__uint_as_float(s[0]), __uint_as_float(s[1]));
    auto t = __builtin_amdgcn_permlane32_swap(__float_as_uint(x), __float_as_uint(x), false, false);
    return fmaxf(__uint_as_float(t[0]), __uint_as_float(t[1]));
}
__device__ __forceinline__ float wave_sum(float v) {
    v += dpp<0xB1>(v); v += dpp<0x4E>(v); v += dpp<0x141>(v); v += dpp<0x140>(v);
    return xrow_sum(v);
}
__device__ __forceinline__ unsigned tag_bad(v4u p, unsigned T) { return (p.y ^ T) | (p.w ^ ~T); }
__device__ __forceinline__ void rbf2(float& a, float& b) { const unsigned p = cvtpk(a, b); a = __uint_as_float(p << 16); b = __uint_as_float(p & 0xffff0000u); }
__device__ __forceinline__ float rcp(float x) { return __builtin_amdgcn_rcpf(x); }
__device__ __forceinline__ v4u mkpair(float a, float b, unsigned T) { return v4u{__float_as_uint(a), T, __float_as_uint(b), ~T}; }

#define SPIN_PAUSE(n) do { __builtin_amdgcn_s_sleep(n); asm volatile("" :: "s"(ws) : "memory"); } while (0)

constexpr int L_XS = 0;
constexpr int L_Q = 4608;
constexpr int L_ACT = 13312 + 8192;
constexpr int L_ML = 6656;
constexpr int L_FLAG = 7040;
constexpr int L_SCR = 7168;
constexpr int L_RED2 = 9216;
constexpr int L_RED = 13312;
constexpr int L_K = L_RED + 16384;
constexpr int L_BYTES = L_K + 131072;

__global__ void __launch_bounds__(768, 3)
fused_kernel(const u16* __restrict__ x, const u16* __restrict__ kc, const u16* __restrict__ vc, const u16* __restrict__ wq, const u16* __restrict__ wo,
             const u16* __restrict__ w1, const u16* __restrict__ b1, const u16* __restrict__ w2, const u16* __restrict__ b2,
             u16* __restrict__ out, unsigned char* ws) {
    __shared__ __attribute__((aligned(16))) unsigned char lds[L_BYTES];
    const int tid = threadIdx.x, lane = tid & 63, w = __builtin_amdgcn_readfirstlane(tid >> 6), bid = blockIdx.x;
#define KARGS asm volatile("" :: "s"(x), "s"(kc), "s"(vc), "s"(wq), "s"(wo), "s"(w1), "s"(b1), "s"(w2), "s"(b2), "s"(out), "s"(ws))
    unsigned pf3;
    {
        unsigned long long pc0; unsigned len;
        asm volatile("s_getpc_b64 %0\n2:\n\ts_mov_b32 %1, 1f-2b" : "=s"(pc0), "=s"(len));
        const __amdgpu_buffer_rsrc_t rc = RSRC(pc0 & ~255ull, len + ((unsigned)pc0 & 255u));
        pf3 = __builtin_amdgcn_raw_buffer_load_b32(rc, tid * 32, 0, 0);
    }
    __builtin_amdgcn_sched_barrier(0);
    volatile LAS unsigned* qflag = (volatile LAS unsigned*)(LAS unsigned char*)(lds + L_FLAG);
    if (tid < 8) qflag[tid] = 0u;
    const int s = bid & 7, b = (bid >> 3) & 1, kvh = bid >> 4;
    const int lrow = lane >> 3, lcol = (lane & 7) * 16;
    const __amdgpu_buffer_rsrc_t rws = RSRC(ws, WS_END);
    int ka[16];
    ka[0] = lane * 16 + ((((w ^ b) & 1) * 8) | (((w >> 1) * 2 + kvh) & 7)) * 1024;
#pragma unroll
    for (int p = 1; p < 16; ++p) ka[p] = ka[0] ^ (p * 1024);
    const unsigned kvo = (unsigned)(((b * HKV + kvh) * SK + (SK - WIN) + s * 512 + (w & 7) * 64) * (DH * 2));
    asm volatile("" : "+v"(ka[0]), "+v"(ka[1]), "+v"(ka[2]), "+v"(ka[3]), "+v"(ka[4]), "+v"(ka[5]), "+v"(ka[6]), "+v"(ka[7]));
    asm volatile("" : "+v"(ka[8]), "+v"(ka[9]), "+v"(ka[10]), "+v"(ka[11]), "+v"(ka[12]), "+v"(ka[13]), "+v"(ka[14]), "+v"(ka[15]));
    KARGS;
#define FRONT_LOADS \
    if (w == 8 || w == 9) { \
        __builtin_amdgcn_s_setprio(3); \
        const __amdgpu_buffer_rsrc_t rx0 = RSRC(x, D * 2), rx1 = RSRC(x + D, D * 2); \
        const int e_k1 = bid / 72, e_c1 = bid - 72 * e_k1; \
        const bool q8 = w == 8; \
        const int ld = q8 ? 4096 * 2 : F * 2; \
        const int colb = q8 ? 512 * kvh + 128 * (bid & 3) : 128 * e_c1; \
        const int rowb = q8 ? 144 * ((bid & 15) >> 2) : 192 * e_k1; \
        const int nrec = q8 ? 144 * 4096 * 2 : (bid < 216 ? 192 * F * 2 : 0); \
        const __amdgpu_buffer_rsrc_t rw = RSRC((const char*)(q8 ? wq : w1) + (unsigned)(rowb * ld), nrec); \
_Pragma("unroll") \
        for (int j = 0; j < 5; ++j) { xr[0][j] = __builtin_amdgcn_raw_buffer_load_b32(rx0, lane * 4 + 256 * j, 0, 0); xr[1][j] = __builtin_amdgcn_raw_buffer_load_b32(rx1, lane * 4 + 256 * j, 0, 0); } \
        int off = lrow * ld + lcol + colb; const int step = 8 * ld; \
_Pragma("unroll") \
        for (int j = 0; j < 24; ++j) { Wf[j] = LDNT(rw, off); off += step; asm volatile("" : "+v"(off)); } \
        ep = __builtin_amdgcn_raw_buffer_load_b32(rws, WS_E, 0, 16); \
    }
    unsigned xr[2][5]; v4u Wf[24];
    unsigned ep = 0;
    FRONT_LOADS
    __builtin_amdgcn_sched_barrier(0);
    __syncthreads();
    __builtin_amdgcn_sched_barrier(0);
#define ROLES \
    int bi2 = bid; asm volatile("" : "+s"(bi2)); \
    int ln2 = lane; asm volatile("" : "+v"(ln2)); \
    const bool role_wo = bi2 < 144, role_w2 = bi2 >= 144 && bi2 < 252; \
    const int u2 = role_wo ? bi2 : bi2 - 144; \
    const int trg = u2 / 9, tcg = u2 - 9 * trg;            \
    const unsigned hm = (ln2 & 1) ? 0xffff0000u : 0x0000ffffu;        \
    const int dsel = (ln2 >> 1) & 3; \
    const unsigned M0 = dsel == 0 ? hm : 0u, M1 = dsel == 1 ? hm : 0u, M2 = dsel == 2 ? hm : 0u, M3 = dsel == 3 ? hm : 0u;
#define BDIAG(dup) v4u{(dup) & M0, (dup) & M1, (dup) & M2, (dup) & M3}
    unsigned T;

    if (__builtin_expect(w < 8, 1)) {
        v4u K[16], V[16];
        {
            const __amdgpu_buffer_rsrc_t rk = RSRC((const char*)kc + kvo, 64 * 256);
            const __amdgpu_buffer_rsrc_t rv = RSRC((const char*)vc + kvo, 64 * 256);
            __builtin_amdgcn_s_setprio(1);
#pragma unroll
            for (int j = 0; j < 16; ++j) K[j] = LDNT(rk, ka[j]);
            __builtin_amdgcn_sched_barrier(0);
            __builtin_amdgcn_s_setprio(0);
            __builtin_amdgcn_sched_barrier(0);
#pragma unroll
            for (int j = 0; j < 16; ++j) V[j] = LDNT(rv, ka[j]);
        }
        __builtin_amdgcn_sched_barrier(0);
        ROLES
        if (lane == 0) __hip_atomic_fetch_add((LAS unsigned*)(qflag + 5), 1u, __ATOMIC_RELAXED, __HIP_MEMORY_SCOPE_WORKGROUP);
        const int g = lane >> 4, c = lane & 15;
#pragma unroll
        for (int j = 0; j < 16; ++j) { const int i = 4 * (j & 3) + g; *(v4u*)(lds + L_K + w * 16384 + (j >> 2) * 4096 + i * 256 + ((c ^ (4 * g + (j & 3))) * 16)) = K[j]; }
        asm volatile("" :: "v"(pf3));
        { unsigned spin = 0; NOUNROLL while (*qflag == 0u && ++spin < (SPIN_CAP << 4)) __builtin_amdgcn_s_sleep(1); T = qflag[1]; }
        asm volatile("s_waitcnt lgkmcnt(0)" ::: "memory");
        v4u qf[4];
#pragma unroll
        for (int ks = 0; ks < 4; ++ks) qf[ks] = *(const v4u*)(lds + L_Q + (c >> 3) * 256 + (4 * ks + g) * 16);
        f32x4 sc[4];
#pragma unroll
        for (int t = 0; t < 4; ++t) {
            sc[t] = f32x4{0, 0, 0, 0};
#pragma unroll
            for (int ks = 0; ks < 4; ++ks) {
                const v4u ka = *(const v4u*)(lds + L_K + w * 16384 + t * 4096 + (4 * (c & 3) + (c >> 2)) * 256 + (((4 * ks + g) ^ c) * 16));
                sc[t] = mfma(ka, qf[ks], sc[t]);
            }
        }
        float m = -INFINITY;
#pragma unroll
        for (int t = 0; t < 4; ++t)
#pragma unroll
            for (int r = 0; r < 4; r += 2) {
                float a0 = sc[t][r], a1 = sc[t][r + 1];
                rbf2(a0, a1); a0 *= 0.088388347648318440f; a1 *= 0.088388347648318440f; rbf2(a0, a1);
                sc[t][r] = a0; sc[t][r + 1] = a1; m = fmaxf(m, fmaxf(a0, a1));
            }
        m = xrow_max(m);
        float l = 0.f;
        constexpr float LOG2E = 1.4426950408889634f;
        const float mm = m * LOG2E;
        unsigned pd[16];
#pragma unroll
        for (int t = 0; t < 4; ++t)
#pragma unroll
            for (int r = 0; r < 4; ++r) { const float p = __builtin_amdgcn_exp2f(sc[t][r] * LOG2E - mm); l += p; pd[4 * t + r] = cvtpk(p, p); }
        l = xrow_sum(l);
        f32x4 acc0 = {0, 0, 0, 0}, acc1 = {0, 0, 0, 0};
#pragma unroll
        for (int t = 0; t < 16; t += 2) { acc0 = mfma(V[t], BDIAG(pd[t]), acc0); acc1 = mfma(V[t + 1], BDIAG(pd[t + 1]), acc1); }
        acc0 += acc1;
        __builtin_amdgcn_s_setprio(3);
        const int hh = (lane >> 3) & 1;
        *(f32x4*)(lds + L_RED + (w * 64 + lane) * 16) = acc0;
        if ((lane & 0x37) == 0) *(float2*)(lds + L_ML + (w * 2 + hh) * 8) = make_float2(mm, l);
        asm volatile("s_waitcnt lgkmcnt(0)" ::: "memory");
        unsigned arr = 0;
        if (lane == 0) arr = __hip_atomic_fetch_add((LAS unsigned*)(qflag + 2), 1u, __ATOMIC_RELAXED, __HIP_MEMORY_SCOPE_WORKGROUP);
        arr = __builtin_amdgcn_readfirstlane(arr);
        asm volatile("" ::: "memory");
        if (arr == 7u) {
            float2 ml[8]; f32x4 pa[8];
#pragma unroll
            for (int i = 0; i < 8; ++i) { ml[i] = *(const float2*)(lds + L_ML + (i * 2 + hh) * 8); pa[i] = *(const f32x4*)(lds + L_RED + (i * 64 + lane) * 16); }
            float M = ml[0].x;
#pragma unroll
            for (int i = 1; i < 8; ++i) M = fmaxf(M, ml[i].x);
            f32x4 a = {0, 0, 0, 0}; float L = 0.f;
#pragma unroll
            for (int i = 0; i < 8; ++i) { const float wt = __builtin_amdgcn_exp2f(ml[i].x - M); a += wt * pa[i]; L += wt * ml[i].y; }
            const int off = WS_AP + bid * AP_REC + lane * 16;
            STSC(mkpair(a[0], a[1], T), rws, off);
            STSC(mkpair(a[2], a[3], T), rws, off + 1024);
            STSC(mkpair(M, L, T), rws, (lane & 0x37) == 0 ? WS_AP + bid * AP_REC + (128 + hh) * 16 : OOB);
            if (lane == 0) qflag[6] = 1u;
        }
        if (role_wo && w < 4) {
            { unsigned spin = 0; NOUNROLL while (qflag[6] == 0u && ++spin < (SPIN_CAP << 4)) __builtin_amdgcn_s_sleep(2); }
            const int bb = (w >> 1) & 1, h2 = w & 1;
            const unsigned bit = 1u << (w & 3);
            const int base = WS_AP + (trg * 16 + bb * 8) * AP_REC;
            v4u g[8], gm[8];
            const int go = base + ((lane >> 5) * 64 + ((lane >> 3) & 3) * 16 + h2 * 8 + (lane & 7)) * 16, gmo = base + (128 + h2) * 16;
            unsigned spin = 0;
            NOUNROLL do {
#pragma unroll
                for (int i = 0; i < 8; ++i) { g[i] = LDSC(rws, go + i * AP_REC); gm[i] = LDSC(rws, gmo + i * AP_REC); }
                unsigned bad = 0;
#pragma unroll
                for (int i = 0; i < 8; ++i) bad |= tag_bad(g[i], T) | tag_bad(gm[i], T);
                if (__all(bad == 0u)) break;
                SPIN_PAUSE(2);
            } while (++spin < SPIN_CAP);
            float M = __uint_as_float(gm[0].x);
#pragma unroll
            for (int i = 1; i < 8; ++i) M = fmaxf(M, __uint_as_float(gm[i].x));
            float a0 = 0.f, a1 = 0.f, L = 0.f;
#pragma unroll
            for (int i = 0; i < 8; ++i) {
                const float wt = __builtin_amdgcn_exp2f(__uint_as_float(gm[i].x) - M);
                a0 += wt * __uint_as_float(g[i].x); a1 += wt * __uint_as_float(g[i].z); L += wt * __uint_as_float(gm[i].z);
            }
            const float ri = rcp(L);
            const int dl = h2 * 128 + 32 * ((lane >> 3) & 3) + 16 * (lane >> 5) + (lane & 7);
            unsigned* act = (unsigned*)(lds + L_ACT);
            act[dl * 2 + bb] = cvtpk(a0 * ri, a0 * ri); act[(dl + 8) * 2 + bb] = cvtpk(a1 * ri, a1 * ri);
            asm volatile("s_waitcnt lgkmcnt(0)" ::: "memory");
            if (lane == 0) __hip_atomic_fetch_or((LAS unsigned*)(qflag + 3), bit, __ATOMIC_RELAXED, __HIP_MEMORY_SCOPE_WORKGROUP);
        }
    } else {
        const int sw = w - 8;
        ROLES
        const int kq = (bi2 & 15) >> 2, cq = bi2 & 3;
        const bool has_w1 = bi2 < 216;
        const int k1 = bi2 / 72, c1 = bi2 - 72 * k1;
        const int trows = role_wo ? 256 : 384;
        const __amdgpu_buffer_rsrc_t rt = RSRC((const char*)(role_wo ? wo : w2) + (unsigned)(trows * trg * (D * 2)), role_wo || role_w2 ? trows * (D * 2) : 0);
        if (sw < 2) {
            const int row0 = sw == 0 ? 144 * kq : 192 * k1;
            {
                float xv[2][10], nv[2][10];
#pragma unroll
                for (int bb = 0; bb < 2; ++bb) {
                    float t = 0.f;
#pragma unroll
                    for (int j = 0; j < 5; ++j) { xv[bb][2 * j] = bf2f(xr[bb][j] & 0xffffu); xv[bb][2 * j + 1] = __uint_as_float(xr[bb][j] & 0xffff0000u); t += xv[bb][2 * j] + xv[bb][2 * j + 1]; }
                    const float mean = wave_sum(t) * (1.f / D);
                    float t8 = 0.f; t = 0.f;
#pragma unroll
                    for (int k = 0; k < 8; ++k) { const float d = xv[bb][k] - mean; t += d * d; }
#pragma unroll
                    for (int k = 8; k < 10; ++k) { const float d = xv[bb][k] - mean; t8 += d * d; }
                    const float var = wave_sum(lane < 32 ? t + t8 : t) * (1.f / D);
                    const float mb = rbf(mean);
                    const float ri = rcp(rbf(__builtin_amdgcn_sqrtf(rbf(rbf(var) + 1e-5f))));
#pragma unroll
                    for (int k = 0; k < 10; k += 2) { float a0 = xv[bb][k] - mb, a1 = xv[bb][k + 1] - mb; rbf2(a0, a1); nv[bb][k] = a0 * ri; nv[bb][k + 1] = a1 * ri; }
                }
#pragma unroll
                for (int j = 0; j < 5; ++j) {
                    const v4u o = {cvtpk(nv[0][2 * j], nv[0][2 * j]), cvtpk(nv[1][2 * j], nv[1][2 * j]), cvtpk(nv[0][2 * j + 1], nv[0][2 * j + 1]), cvtpk(nv[1][2 * j + 1], nv[1][2 * j + 1])};
                    if (j < 4 || lane < 32) *(v4u*)(lds + L_XS + (lane + 64 * j) * 16) = o;
                }
            }
            __builtin_amdgcn_fence(__ATOMIC_ACQ_REL, "wavefront");
            asm volatile("" ::: "memory");
            const v2u* xs = (const v2u*)(lds + L_XS) + row0 + lrow;
            f32x4 af[2] = {{0, 0, 0, 0}, {0, 0, 0, 0}}, ag[2] = {{0, 0, 0, 0}, {0, 0, 0, 0}};
#pragma unroll
            for (int j = 0; j < 18; ++j) {
                const v2u d = xs[8 * j];
                if (j & 1) { ag[0] = mfma(Wf[j], BDIAG(d.x), ag[0]); ag[1] = mfma(Wf[j], BDIAG(d.y), ag[1]); } else { af[0] = mfma(Wf[j], BDIAG(d.x), af[0]); af[1] = mfma(Wf[j], BDIAG(d.y), af[1]); }
            }
            if (sw == 1) {
#pragma unroll
                for (int j = 18; j < 24; ++j) {
                    const v2u d = xs[8 * j];
                    if (j & 1) { ag[0] = mfma(Wf[j], BDIAG(d.x), ag[0]); ag[1] = mfma(Wf[j], BDIAG(d.y), ag[1]); } else { af[0] = mfma(Wf[j], BDIAG(d.x), af[0]); af[1] = mfma(Wf[j], BDIAG(d.y), af[1]); }
                }
            }
            af[0] += ag[0]; af[1] += ag[1];
            T = __builtin_amdgcn_readfirstlane(ep) + 1u;
            float* scr = (float*)(lds + L_SCR + sw * 1024);
            if ((lane >> 5) == ((lane >> 3) & 1)) {
                const int r1 = lane >> 5, cb = 32 * ((lane >> 4) & 1) + (lane & 7);
#pragma unroll
                for (int bb = 0; bb < 2; ++bb)
#pragma unroll
                    for (int r = 0; r < 4; ++r) scr[(r1 * 2 + bb) * 64 + cb + 8 * r] = af[bb][r];
            }
            __builtin_amdgcn_fence(__ATOMIC_ACQ_REL, "wavefront");
            asm volatile("" ::: "memory");
            {
                const int bb = lane >> 5, pr = lane & 31;
                const float2 t0 = *(const float2*)&scr[bb * 64 + 2 * pr], t1 = *(const float2*)&scr[(2 + bb) * 64 + 2 * pr];
                const int off = sw == 0 ? WS_QP + (((kvh * 2 + bb) * 4 + kq) * 128 + cq * 32 + pr) * 16
                                        : (has_w1 ? WS_HP + (((c1 * 2 + bb) * 3 + k1) * 32 + pr) * 16 : OOB);
                STSC(mkpair(t0.x + t1.x, t0.y + t1.y, T), rws, off);
            }
            if (sw == 0) {
                const int qoff = WS_QP + ((kvh * 2 + b) * 4 * 128 + lane) * 16;
                v4u gq[8]; unsigned spin = 0;
                NOUNROLL do {
#pragma unroll
                    for (int i = 0; i < 8; ++i) gq[i] = LDSC(rws, qoff + i * 1024);
                    unsigned bad = 0;
#pragma unroll
                    for (int i = 0; i < 8; ++i) bad |= tag_bad(gq[i], T);
                    if (__all(bad == 0u)) break;
                    SPIN_PAUSE(1);
                } while (++spin < SPIN_CAP);
#pragma unroll
                for (int hf2 = 0; hf2 < 2; ++hf2) {
                    const float q0 = (__uint_as_float(gq[hf2].x) + __uint_as_float(gq[2 + hf2].x)) + (__uint_as_float(gq[4 + hf2].x) + __uint_as_float(gq[6 + hf2].x));
                    const float q1 = (__uint_as_float(gq[hf2].z) + __uint_as_float(gq[2 + hf2].z)) + (__uint_as_float(gq[4 + hf2].z) + __uint_as_float(gq[6 + hf2].z));
                    ((unsigned*)(lds + L_Q))[hf2 * 64 + lane] = cvtpk(q0, q1);
                }
                if (lane == 0) qflag[1] = T;
                asm volatile("s_waitcnt lgkmcnt(0)" ::: "memory");
                if (lane == 0) *qflag = 1u;
            }
            __builtin_amdgcn_s_setprio(0);
        }
        __builtin_amdgcn_sched_barrier(0);
        asm volatile("" :: "v"(pf3));
        { unsigned spin = 0; NOUNROLL while (qflag[5] < 8u && ++spin < (SPIN_CAP << 4)) __builtin_amdgcn_s_sleep(2); }
        v4u Wt[12];
#pragma unroll
        for (int j = 0; j < 12; ++j) Wt[j] = LDNT(rt, ((8 * sw + lrow) * D + 64 * tcg) * 2 + lcol + j * (32 * D * 2));
        __builtin_amdgcn_sched_barrier(0);
        if (sw >= 2 || (sw == 1 && !has_w1)) {
            unsigned spin = 0; NOUNROLL while (*qflag == 0u && ++spin < (SPIN_CAP << 4)) __builtin_amdgcn_s_sleep(4);
            T = qflag[1];
        }
        if (role_w2) {
            if (sw) {
                const int hpr = (sw - 1) * 64 + lane, hf = 384 * trg + 2 * hpr;
                const int hoff = WS_HP + ((hf >> 6) * 6 * 32 + ((hf & 63) >> 1)) * 16;
                const unsigned b1v = *(const unsigned*)(b1 + hf);
                v4u gh[6]; unsigned spin = 0;
                NOUNROLL do {
#pragma unroll
                    for (int i = 0; i < 6; ++i) gh[i] = LDSC(rws, hoff + i * 512);
                    unsigned bad = 0;
#pragma unroll
                    for (int i = 0; i < 6; ++i) bad |= tag_bad(gh[i], T);
                    if (__all(bad == 0u)) break;
                    SPIN_PAUSE(2);
                } while (++spin < SPIN_CAP);
                float hv[4];
#pragma unroll
                for (int e = 0; e < 2; ++e) {
                    float h0 = __uint_as_float(gh[3 * e].x) + __uint_as_float(gh[3 * e + 1].x) + __uint_as_float(gh[3 * e + 2].x);
                    float h1 = __uint_as_float(gh[3 * e].z) + __uint_as_float(gh[3 * e + 1].z) + __uint_as_float(gh[3 * e + 2].z);
                    rbf2(h0, h1); h0 += bf2f(b1v & 0xffffu); h1 += __uint_as_float(b1v & 0xffff0000u); rbf2(h0, h1);
                    h0 = h0 > 0.f ? h0 : 0.f; h1 = h1 > 0.f ? h1 : 0.f;
                    hv[e] = h0 * h0; hv[2 + e] = h1 * h1;
                }
                *(v4u*)(lds + L_ACT + hpr * 16) = v4u{cvtpk(hv[0], hv[0]), cvtpk(hv[1], hv[1]), cvtpk(hv[2], hv[2]), cvtpk(hv[3], hv[3])};
            }
            asm volatile("s_waitcnt lgkmcnt(0)" ::: "memory");
            if (lane == 0) __hip_atomic_fetch_or((LAS unsigned*)(qflag + 3), 1u << sw, __ATOMIC_RELAXED, __HIP_MEMORY_SCOPE_WORKGROUP);
        }
        if (role_wo || role_w2) {
            { unsigned spin = 0; NOUNROLL while (qflag[3] != 15u && ++spin < (SPIN_CAP << 4)) __builtin_amdgcn_s_sleep(0); }
            asm volatile("" ::: "memory");
            const unsigned* act = (const unsigned*)(lds + L_ACT);
            f32x4 at[2][2] = {{{0, 0, 0, 0}, {0, 0, 0, 0}}, {{0, 0, 0, 0}, {0, 0, 0, 0}}};
            v2u ad[8];
#pragma unroll
            for (int j = 0; j < 8; ++j) ad[j] = *(const v2u*)&act[(8 * (sw + 4 * j) + lrow) * 2];
#pragma unroll
            for (int j = 0; j < 8; ++j) { at[0][j & 1] = mfma(Wt[j], BDIAG(ad[j].x), at[0][j & 1]); at[1][j & 1] = mfma(Wt[j], BDIAG(ad[j].y), at[1][j & 1]); }
            if (role_w2) {
#pragma unroll
                for (int j = 8; j < 12; ++j) { const v2u a2 = *(const v2u*)&act[(8 * (sw + 4 * j) + lrow) * 2]; at[0][j & 1] = mfma(Wt[j], BDIAG(a2.x), at[0][j & 1]); at[1][j & 1] = mfma(Wt[j], BDIAG(a2.y), at[1][j & 1]); }
            }
            at[0][0] += at[0][1]; at[1][0] += at[1][1];
            float* red2 = (float*)(lds + L_RED2);
            if ((lane >> 5) == ((lane >> 3) & 1)) {
                const int r1 = lane >> 5, cb = 32 * ((lane >> 4) & 1) + (lane & 7);
#pragma unroll
                for (int bb = 0; bb < 2; ++bb)
#pragma unroll
                    for (int r = 0; r < 4; ++r) red2[((sw * 2 + r1) * 2 + bb) * 64 + cb + 8 * r] = at[bb][0][r];
            }
            asm volatile("s_waitcnt lgkmcnt(0)" ::: "memory");
            unsigned arr = 0;
            if (lane == 0) arr = __hip_atomic_fetch_add((LAS unsigned*)(qflag + 4), 1u, __ATOMIC_RELAXED, __HIP_MEMORY_SCOPE_WORKGROUP);
            arr = __builtin_amdgcn_readfirstlane(arr);
            asm volatile("" ::: "memory");
            if (arr == 3u) {
                const int bb = lane >> 5, pr = lane & 31;
                float v0 = 0.f, v1 = 0.f;
#pragma unroll
                for (int i = 0; i < 8; ++i) { const float2 t = *(const float2*)&red2[(i * 2 + bb) * 64 + 2 * pr]; v0 += t.x; v1 += t.y; }
                const int off = role_wo ? WS_SP + (((tcg * 2 + bb) * 16 + trg) * 32 + pr) * 16 : WS_MP + (((tcg * 2 + bb) * 12 + trg) * 32 + pr) * 16;
                STSC(mkpair(v0, v1, T), rws, off);
            }
        }
        asm volatile("1:");
    }

    if (bid >= 144 && bid < 216 && w == 1) {
        const int u = bid - 144, cg = u >> 3, fb = (u >> 2) & 1, q4 = u & 3;
        const int p8 = lane >> 3, j = lane & 7;
        const int col = 64 * cg + 16 * q4 + 2 * j;
        const unsigned xw = *(const unsigned*)(x + fb * D + col), bw = *(const unsigned*)(b2 + col);
        const int offs = WS_SP + (((cg * 2 + fb) * 16 + p8) * 32 + 8 * q4 + j) * 16;
        const int offm = WS_MP + (((cg * 2 + fb) * 12 + p8) * 32 + 8 * q4 + j) * 16;
        v4u g[4]; unsigned spin = 0;
        NOUNROLL do {
            g[0] = LDSC(rws, offs); g[1] = LDSC(rws, offs + 8 * 32 * 16); g[2] = LDSC(rws, offm); g[3] = LDSC(rws, p8 < 4 ? offm + 8 * 32 * 16 : OOB);
            const unsigned bad = tag_bad(g[0], T) | tag_bad(g[1], T) | tag_bad(g[2], T) | (p8 < 4 ? tag_bad(g[3], T) : 0u);
            if (__all(bad == 0u)) break;
            SPIN_PAUSE(1);
        } while (++spin < SPIN_CAP);
        float s0 = __uint_as_float(g[0].x) + __uint_as_float(g[1].x), s1 = __uint_as_float(g[0].z) + __uint_as_float(g[1].z);
        float m0 = __uint_as_float(g[2].x) + __uint_as_float(g[3].x), m1 = __uint_as_float(g[2].z) + __uint_as_float(g[3].z);
        s0 += dpp<0x128>(s0); s1 += dpp<0x128>(s1); m0 += dpp<0x128>(m0); m1 += dpp<0x128>(m1);
        s0 = xrow_sum(s0); s1 = xrow_sum(s1); m0 = xrow_sum(m0); m1 = xrow_sum(m1);
        if (lane < 8) {
            const float mv0 = rbf(rbf(m0) + bf2f(bw & 0xffffu)), mv1 = rbf(rbf(m1) + __uint_as_float(bw & 0xffff0000u));
            const float g0 = rbf(mv0 * rcp(1.f + __expf(-mv0))), g1 = rbf(mv1 * rcp(1.f + __expf(-mv1)));
            *(unsigned*)(out + fb * D + col) = cvtpk(bf2f(xw & 0xffffu) + rbf(rbf(s0) * g0), __uint_as_float(xw & 0xffff0000u) + rbf(rbf(s1) * g1));
        }
        if (u == 0 && lane == 0) __builtin_amdgcn_raw_buffer_store_b32(T, rws, WS_E, 0, 16);
    }
}

extern "C" void kernel_launch(void* const* d_in, const int* in_sizes, int n_in, void* d_out, int out_size, void* d_ws, size_t ws_size, hipStream_t stream) {
    fused_kernel<<<256, 768, 0, stream>>>((const u16*)d_in[0], (const u16*)d_in[1], (const u16*)d_in[2], (const u16*)d_in[3], (const u16*)d_in[4],
                                          (const u16*)d_in[6], (const u16*)d_in[7], (const u16*)d_in[8], (const u16*)d_in[9], (u16*)d_out, (unsigned char*)d_ws);
}
```
